```python
import torch

def _softmax(x, axis=-1):
    return torch.softmax(x, dim=axis)
def _gelu(x):
    return torch.nn.functional.gelu(x, approximate="tanh")
def _silu(x):
    return torch.nn.functional.silu(x)
def _sigmoid(x):
    return torch.sigmoid(x)
def _layernorm(x, axis=-1, eps=1e-5):
    m = x.mean(dim=axis, keepdim=True)
    v = x.var(dim=axis, keepdim=True, unbiased=False)
    return (x - m) / torch.sqrt(v + eps)
def _scatter_add(tgt, idx, src, axis=0):
    return tgt.clone().index_add_(axis, idx, src)
def _segment_sum(src, seg, n):
    out = torch.zeros((n,) + src.shape[1:], dtype=src.dtype)
    return out.index_add_(0, seg, src)
def _pad(x, pw):
    flat = tuple(v for pair in reversed(pw) for v in pair)
    return torch.nn.functional.pad(x, flat)

kernel_name = 'standalone__glu_gated_bilinear__decode'

def setup_inputs():

    _w = (lambda a: (lambda t: t.to(torch.bfloat16) if t.is_floating_point() else t)(torch.as_tensor(a)))
    B, S, D, F = 1, 1, 1024, 3584
    return {
        "x": _w((lambda *s: torch.randn(*s, dtype=torch.bfloat16) * 0.05)(B, S, D)),
        "w_gate": _w((lambda *s: torch.randn(*s, dtype=torch.bfloat16) * 0.05)(D, F) * (D ** -0.5)),
        "w_up": _w((lambda *s: torch.randn(*s, dtype=torch.bfloat16) * 0.05)(D, F) * (D ** -0.5)),
        "w_down": _w((lambda *s: torch.randn(*s, dtype=torch.bfloat16) * 0.05)(F, D) * (F ** -0.5)),
    }

def reference(x, w_gate, w_up, w_down):
    g = torch.einsum("bsd,df->bsf", x, w_gate)
    u = torch.einsum("bsd,df->bsf", x, w_up)
    h = g * u
    return torch.einsum("bsf,fd->bsd", h, w_down)
```

```cpp
#include <hip/hip_runtime.h>
#include <hip/hip_bf16.h>

#define GAS __attribute__((address_space(1)))
typedef unsigned v4u __attribute__((ext_vector_type(4)));
typedef unsigned v2u __attribute__((ext_vector_type(2)));
typedef float v2f __attribute__((ext_vector_type(2)));
typedef __bf16 bf16x2 __attribute__((ext_vector_type(2)));

constexpr int D = 1024, F = 3584;
constexpr int NP = 224;
constexpr int NT = 512;

__device__ __forceinline__ float dot2(unsigned a, unsigned b, float c) {
    return __builtin_amdgcn_fdot2_f32_bf16(__builtin_bit_cast(bf16x2, a), __builtin_bit_cast(bf16x2, b), c, false);
}
__device__ __forceinline__ unsigned lo2(unsigned a, unsigned b) { return __builtin_amdgcn_perm(b, a, 0x05040100u); }
__device__ __forceinline__ unsigned hi2(unsigned a, unsigned b) { return __builtin_amdgcn_perm(b, a, 0x07060302u); }
__device__ __forceinline__ float rbf(float f) { return __bfloat162float(__float2bfloat16(f)); }

__global__ void __launch_bounds__(NT, 2) k_part(const unsigned short* __restrict__ x, const unsigned short* __restrict__ wg,
                                             const unsigned short* __restrict__ wu, const unsigned short* __restrict__ wd,
                                             float* __restrict__ part) {
    __shared__ float red[8][32];
    const int b = blockIdx.x, t = threadIdx.x, lane = t & 63, wave = t >> 6;
    const int slice = (b & 7) * 28 + (b >> 3);
    const int f0 = slice * 16;
    const int half = t & 1, rq = t >> 1;
    const GAS v4u* pg = (const GAS v4u*)(wg + (size_t)(4 * rq) * F + f0 + 8 * half);
    const GAS v4u* pu = (const GAS v4u*)(wu + (size_t)(4 * rq) * F + f0 + 8 * half);
    v4u g[4], u[4];
    const v2u xv = *(const GAS v2u*)(x + 4 * rq);
#pragma unroll
    for (int i = 0; i < 4; ++i) { g[i] = pg[i * (F / 8)]; u[i] = pu[i * (F / 8)]; }
    unsigned wdv[16];
    const GAS unsigned* pd = (const GAS unsigned*)(wd + (size_t)f0 * D) + t;
#pragma unroll
    for (int r = 0; r < 16; ++r) wdv[r] = pd[r * (D / 2)];
    __builtin_amdgcn_sched_barrier(0);
    float a[16];
#pragma unroll
    for (int j = 0; j < 4; ++j) {
        a[2 * j] = dot2(lo2(g[0][j], g[1][j]), xv.x, 0.f);
        a[2 * j + 1] = dot2(hi2(g[0][j], g[1][j]), xv.x, 0.f);
        a[8 + 2 * j] = dot2(lo2(u[0][j], u[1][j]), xv.x, 0.f);
        a[8 + 2 * j + 1] = dot2(hi2(u[0][j], u[1][j]), xv.x, 0.f);
    }
#pragma unroll
    for (int j = 0; j < 4; ++j) {
        a[2 * j] = dot2(lo2(g[2][j], g[3][j]), xv.y, a[2 * j]);
        a[2 * j + 1] = dot2(hi2(g[2][j], g[3][j]), xv.y, a[2 * j + 1]);
        a[8 + 2 * j] = dot2(lo2(u[2][j], u[3][j]), xv.y, a[8 + 2 * j]);
        a[8 + 2 * j + 1] = dot2(hi2(u[2][j], u[3][j]), xv.y, a[8 + 2 * j + 1]);
    }
#pragma unroll
    for (int m = 2; m <= 32; m <<= 1)
#pragma unroll
        for (int j = 0; j < 16; ++j) a[j] += __shfl_xor(a[j], m);
    if (lane < 2) {
#pragma unroll
        for (int j = 0; j < 8; ++j) { red[wave][8 * lane + j] = a[j]; red[wave][16 + 8 * lane + j] = a[8 + j]; }
    }
    __syncthreads();
    float s = 0.f;
#pragma unroll
    for (int w = 0; w < 8; ++w) s += red[w][lane & 31];
    s = rbf(s);
    const float o = __shfl_xor(s, 16);
    const float h = rbf(s * o);
    const unsigned hb = __builtin_bit_cast(unsigned, h) >> 16;
    const unsigned hn = __shfl_xor(hb, 1);
    const unsigned hp = hb | (hn << 16);
    float o0 = 0.f, o1 = 0.f;
#pragma unroll
    for (int r = 0; r < 16; r += 2) {
        const unsigned hh = __builtin_amdgcn_readlane(hp, r);
        o0 = dot2(lo2(wdv[r], wdv[r + 1]), hh, o0);
        o1 = dot2(hi2(wdv[r], wdv[r + 1]), hh, o1);
    }
    ((GAS v2f*)part)[(size_t)slice * (D / 2) + t] = v2f{o0, o1};
}

__global__ void __launch_bounds__(512) k_red(const float* __restrict__ part, __hip_bfloat16* __restrict__ out) {
    __shared__ float red[32][16];
    const int t = threadIdx.x, c = t & 15, pg = t >> 4;
    float s = 0.f;
#pragma unroll
    for (int i = 0; i < 7; ++i) s += part[(size_t)(pg + 32 * i) * D + blockIdx.x * 16 + c];
    red[pg][c] = s;
    __syncthreads();
    if (t < 16) {
        float v = 0.f;
#pragma unroll
        for (int i = 0; i < 32; ++i) v += red[i][t];
        out[blockIdx.x * 16 + t] = __float2bfloat16(v);
    }
}

extern "C" void kernel_launch(void* const* d_in, const int* in_sizes, int n_in,
                              void* d_out, int out_size, void* d_ws, size_t ws_size,
                              hipStream_t stream) {
    const unsigned short* x = (const unsigned short*)d_in[0];
    const unsigned short* wg = (const unsigned short*)d_in[1];
    const unsigned short* wu = (const unsigned short*)d_in[2];
    const unsigned short* wd = (const unsigned short*)d_in[3];
    float* part = (float*)d_ws;
    k_part<<<NP, NT, 0, stream>>>(x, wg, wu, wd, part);
    k_red<<<64, 512, 0, stream>>>(part, (__hip_bfloat16*)d_out);
}
```

```cpp
#include <hip/hip_runtime.h>
#include <hip/hip_bf16.h>

#define GAS __attribute__((address_space(1)))
typedef unsigned v4u __attribute__((ext_vector_type(4)));
typedef unsigned v2u __attribute__((ext_vector_type(2)));
typedef __bf16 bf16x2 __attribute__((ext_vector_type(2)));
#define RLX_AGENT __ATOMIC_RELAXED, __HIP_MEMORY_SCOPE_AGENT

constexpr int D = 1024, F = 3584;
constexpr int NP = 224;
constexpr int NT = 512;
constexpr int NR = 64;
constexpr unsigned TAGC = 0x9E3779B9u;
constexpr int EP_STRIDE = 64;
constexpr size_t INBOX_OFF = 65536;
constexpr int AUX_SC1 = 16;

__device__ __forceinline__ float dot2(unsigned a, unsigned b, float c) {
    return __builtin_amdgcn_fdot2_f32_bf16(__builtin_bit_cast(bf16x2, a), __builtin_bit_cast(bf16x2, b), c, false);
}
__device__ __forceinline__ unsigned lo2(unsigned a, unsigned b) { return __builtin_amdgcn_perm(b, a, 0x05040100u); }
__device__ __forceinline__ unsigned hi2(unsigned a, unsigned b) { return __builtin_amdgcn_perm(b, a, 0x07060302u); }
__device__ __forceinline__ float rbf(float f) { return __bfloat162float(__float2bfloat16(f)); }

__global__ void __launch_bounds__(NT, 2) k_fused(const unsigned short* __restrict__ x, const unsigned short* __restrict__ wg,
                                              const unsigned short* __restrict__ wu, const unsigned short* __restrict__ wd,
                                              unsigned char* ws, unsigned short* __restrict__ out) {
    __shared__ float red[8][32];
    const int b = blockIdx.x, t = threadIdx.x, lane = t & 63, wave = t >> 6;
    const int slice = (b & 7) * 28 + (b >> 3);
    const int f0 = slice * 16;
    const int half = t & 1, rq = t >> 1;
    const GAS v4u* pg = (const GAS v4u*)(wg + (size_t)(4 * rq) * F + f0 + 8 * half);
    const GAS v4u* pu = (const GAS v4u*)(wu + (size_t)(4 * rq) * F + f0 + 8 * half);
    v4u g[4], u[4];
    const v2u xv = *(const GAS v2u*)(x + 4 * rq);
#pragma unroll
    for (int i = 0; i < 4; ++i) { g[i] = pg[i * (F / 8)]; u[i] = pu[i * (F / 8)]; }
    unsigned wdv[16];
    const GAS unsigned* pd = (const GAS unsigned*)(wd + (size_t)f0 * D) + t;
#pragma unroll
    for (int r = 0; r < 16; ++r) wdv[r] = __builtin_nontemporal_load(pd + r * (D / 2));
    const unsigned tagp = __hip_atomic_load((GAS unsigned*)ws + (t >> 3) * EP_STRIDE, RLX_AGENT) + TAGC;
    const unsigned tag = __hip_atomic_load((GAS unsigned*)ws + (b & (NR - 1)) * EP_STRIDE, RLX_AGENT) + TAGC;
    __builtin_amdgcn_sched_barrier(0);
    float a[16];
#pragma unroll
    for (int j = 0; j < 4; ++j) {
        a[2 * j] = dot2(lo2(g[0][j], g[1][j]), xv.x, 0.f);
        a[2 * j + 1] = dot2(hi2(g[0][j], g[1][j]), xv.x, 0.f);
        a[8 + 2 * j] = dot2(lo2(u[0][j], u[1][j]), xv.x, 0.f);
        a[8 + 2 * j + 1] = dot2(hi2(u[0][j], u[1][j]), xv.x, 0.f);
    }
#pragma unroll
    for (int j = 0; j < 4; ++j) {
        a[2 * j] = dot2(lo2(g[2][j], g[3][j]), xv.y, a[2 * j]);
        a[2 * j + 1] = dot2(hi2(g[2][j], g[3][j]), xv.y, a[2 * j + 1]);
        a[8 + 2 * j] = dot2(lo2(u[2][j], u[3][j]), xv.y, a[8 + 2 * j]);
        a[8 + 2 * j + 1] = dot2(hi2(u[2][j], u[3][j]), xv.y, a[8 + 2 * j + 1]);
    }
#pragma unroll
    for (int m = 2; m <= 32; m <<= 1)
#pragma unroll
        for (int j = 0; j < 16; ++j) a[j] += __shfl_xor(a[j], m);
    if (lane < 2) {
#pragma unroll
        for (int j = 0; j < 8; ++j) { red[wave][8 * lane + j] = a[j]; red[wave][16 + 8 * lane + j] = a[8 + j]; }
    }
    __syncthreads();
    float s = 0.f;
#pragma unroll
    for (int w = 0; w < 8; ++w) s += red[w][lane & 31];
    s = rbf(s);
    const float o = __shfl_xor(s, 16);
    const float h = rbf(s * o);
    const unsigned hb = __builtin_bit_cast(unsigned, h) >> 16;
    const unsigned hn = __shfl_xor(hb, 1);
    const unsigned hp = hb | (hn << 16);
    float o0 = 0.f, o1 = 0.f;
#pragma unroll
    for (int r = 0; r < 16; r += 2) {
        const unsigned hh = __builtin_amdgcn_readlane(hp, r);
        o0 = dot2(lo2(wdv[r], wdv[r + 1]), hh, o0);
        o1 = dot2(hi2(wdv[r], wdv[r + 1]), hh, o1);
    }
    const __amdgpu_buffer_rsrc_t rs = __builtin_amdgcn_make_buffer_rsrc(ws + INBOX_OFF, 0, NR * NP * 128, 0x00020000);
    {
        const v4u v = {__builtin_bit_cast(unsigned, o0), tagp, __builtin_bit_cast(unsigned, o1), tagp};
        __builtin_amdgcn_raw_buffer_store_b128(v, rs, ((t >> 3) * NP + slice) * 128 + (t & 7) * 16, 0, AUX_SC1);
    }
    if (b >= NR) return;
    float s0 = 0.f, s1 = 0.f;
    {
        const int base = b * NP * 128 + t * 16;
        v4u v[4];
        bool ok[4];
        const bool has3 = t < (NP - 192) * 8;
        for (unsigned spin = 0; spin < (1u << 16); ++spin) {
#pragma unroll
            for (int i = 0; i < 3; ++i) v[i] = __builtin_amdgcn_raw_buffer_load_b128(rs, base + i * 8192, 0, AUX_SC1);
            v[3] = v4u{0u, tag, 0u, tag};
            if (has3) v[3] = __builtin_amdgcn_raw_buffer_load_b128(rs, base + 3 * 8192, 0, AUX_SC1);
            bool all = true;
#pragma unroll
            for (int i = 0; i < 4; ++i) { ok[i] = (v[i].y == tag) & (v[i].w == tag); all &= ok[i]; }
            if (__all(all)) break;
            __builtin_amdgcn_s_sleep(1);
        }
#pragma unroll
        for (int i = 0; i < 4; ++i) { s0 += __uint_as_float(v[i].x); s1 += __uint_as_float(v[i].z); }
    }
#pragma unroll
    for (int m = 8; m <= 32; m <<= 1) { s0 += __shfl_xor(s0, m); s1 += __shfl_xor(s1, m); }
    __syncthreads();
    if (lane < 8) { red[wave][2 * lane] = s0; red[wave][2 * lane + 1] = s1; }
    __syncthreads();
    if (t < 16) {
        float v = 0.f;
#pragma unroll
        for (int w = 0; w < 8; ++w) v += red[w][t];
        out[b * 16 + t] = __builtin_bit_cast(unsigned short, __float2bfloat16(v));
        if (t == 0) __hip_atomic_store((GAS unsigned*)ws + b * EP_STRIDE, tag, RLX_AGENT);
    }
}

extern "C" void kernel_launch(void* const* d_in, const int* in_sizes, int n_in,
                              void* d_out, int out_size, void* d_ws, size_t ws_size,
                              hipStream_t stream) {
    const unsigned short* x = (const unsigned short*)d_in[0];
    const unsigned short* wg = (const unsigned short*)d_in[1];
    const unsigned short* wu = (const unsigned short*)d_in[2];
    const unsigned short* wd = (const unsigned short*)d_in[3];
    k_fused<<<NP, NT, 0, stream>>>(x, wg, wu, wd, (unsigned char*)d_ws, (unsigned short*)d_out);
}
```

```cpp
#include <hip/hip_runtime.h>
#include <hip/hip_bf16.h>

#define GAS __attribute__((address_space(1)))
typedef unsigned v4u __attribute__((ext_vector_type(4)));
typedef unsigned v2u __attribute__((ext_vector_type(2)));
typedef float v4f __attribute__((ext_vector_type(4)));
typedef float v2f __attribute__((ext_vector_type(2)));
typedef __bf16 bf16x8 __attribute__((ext_vector_type(8)));
#define RLX_AGENT __ATOMIC_RELAXED, __HIP_MEMORY_SCOPE_AGENT

constexpr int D = 1024, F = 3584;
constexpr int NWG = 224, NT = 512;
constexpr unsigned TAGC = 0x9E3779B9u;
constexpr int X1_OFF = 4096;
constexpr int X2_OFF = X1_OFF + NWG * 1024;
constexpr int WS_BYTES = X2_OFF + 64 * 56 * 128;
constexpr int AUX_SC1 = 16, AUX_NT = 2;
constexpr unsigned SPIN_CAP = 1u << 16;

__device__ __forceinline__ v4f mfma(v4u a, v4u b, v4f c) {
    return __builtin_amdgcn_mfma_f32_16x16x32_bf16(__builtin_bit_cast(bf16x8, a), __builtin_bit_cast(bf16x8, b), c, 0, 0, 0);
}
__device__ __forceinline__ float rbf(float f) { return __bfloat162float(__float2bfloat16(f)); }
__device__ __forceinline__ unsigned bfbits(float f) { return (unsigned)__builtin_bit_cast(unsigned short, __float2bfloat16(f)); }

__global__ void __launch_bounds__(NT, 2) k_fused(const unsigned short* __restrict__ x, const unsigned short* __restrict__ wg,
                                              const unsigned short* __restrict__ wu, const unsigned short* __restrict__ wd,
                                              unsigned char* ws, unsigned short* __restrict__ out) {
    __shared__ __attribute__((aligned(16))) float red[8 * 128];
    __shared__ __attribute__((aligned(16))) unsigned hb[32];
    const int b = blockIdx.x, t = threadIdx.x, lane = t & 63;
    const int w = __builtin_amdgcn_readfirstlane(t >> 6);
    const int idx = (b & 7) * 28 + (b >> 3);
    const int cg = idx >> 3, sub = idx & 7, m = sub & 1, q = sub >> 1;
    const int g = lane >> 4, c = lane & 15;

    const __amdgpu_buffer_rsrc_t rw = __builtin_amdgcn_make_buffer_rsrc((void*)(m ? wu : wg), 0, D * F * 2, 0x00020000);
    const __amdgpu_buffer_rsrc_t rd = __builtin_amdgcn_make_buffer_rsrc((void*)wd, 0, D * F * 2, 0x00020000);
    const __amdgpu_buffer_rsrc_t rs = __builtin_amdgcn_make_buffer_rsrc((void*)ws, 0, WS_BYTES, 0x00020000);

    const int row0 = 256 * q + 8 * (4 * w + g);
    const v4u xv = *(const GAS v4u*)(x + row0);
    unsigned tag = __hip_atomic_load((GAS unsigned*)ws, RLX_AGENT) + TAGC;
    __builtin_amdgcn_sched_barrier(0);
    v4u a[8];
    {
        const int vo = row0 * (F * 2) + cg * 256 + c * 16;
#pragma unroll
        for (int i = 0; i < 8; ++i) a[i] = __builtin_amdgcn_raw_buffer_load_b128(rw, vo, i * (F * 2), AUX_NT);
    }
    __builtin_amdgcn_sched_barrier(0);
    __builtin_amdgcn_s_barrier();
    v4u dw[4];
    const int ch = w & 1, rg = w >> 1;
    {
        const int r = 128 * cg + 64 * m + 32 * (rg >> 1) + 4 * (rg & 1) + g;
        const int vo = r * (D * 2) + q * 512 + ch * 256 + c * 16;
#pragma unroll
        for (int i = 0; i < 4; ++i) dw[i] = __builtin_amdgcn_raw_buffer_load_b128(rd, vo, i * (8 * D * 2), AUX_NT);
    }
    __builtin_amdgcn_sched_barrier(0);
    unsigned sel0[4], sel1[4];
#pragma unroll
    for (int jj = 0; jj < 4; ++jj) {
        const bool on = (c >> 1) == jj;
        sel0[jj] = on ? ((c & 1) ? 0x01000c0cu : 0x0c0c0100u) : 0x0c0c0c0cu;
        sel1[jj] = on ? ((c & 1) ? 0x03020c0cu : 0x0c0c0302u) : 0x0c0c0c0cu;
    }
    v4f acc = {0.f, 0.f, 0.f, 0.f};
#pragma unroll
    for (int i = 0; i < 8; ++i) {
        const unsigned xw = xv[i >> 1];
        v4u bb;
#pragma unroll
        for (int jj = 0; jj < 4; ++jj) bb[jj] = __builtin_amdgcn_perm(xw, xw, (i & 1) ? sel1[jj] : sel0[jj]);
        acc = mfma(a[i], bb, acc);
    }
    if (c < 8) *(v4f*)&red[w * 128 + 32 * g + 4 * c] = acc;
    asm volatile("" : "+v"(tag));
    __syncthreads();
    if (w == 0) {
        v2f s = *(const v2f*)&red[2 * lane];
#pragma unroll
        for (int k = 1; k < 8; ++k) s += *(const v2f*)&red[k * 128 + 2 * lane];
        const v4u v = {__float_as_uint(s.x), tag, __float_as_uint(s.y), tag};
        __builtin_amdgcn_raw_buffer_store_b128(v, rs, X1_OFF + idx * 1024 + lane * 16, 0, AUX_SC1);
        const int vo = X1_OFF + (cg * 8 + (lane >> 5)) * 1024 + m * 512 + (lane & 31) * 16;
        v4u p[4];
        for (unsigned spin = 0; spin < SPIN_CAP; ++spin) {
#pragma unroll
            for (int j = 0; j < 4; ++j) p[j] = __builtin_amdgcn_raw_buffer_load_b128(rs, vo, j * 2048, AUX_SC1);
            bool ok = true;
#pragma unroll
            for (int j = 0; j < 4; ++j) ok &= (p[j].y == tag) & (p[j].w == tag);
            if (__all(ok)) break;
            __builtin_amdgcn_s_sleep(1);
        }
        const float s0 = rbf((__uint_as_float(p[0].x) + __uint_as_float(p[1].x)) + (__uint_as_float(p[2].x) + __uint_as_float(p[3].x)));
        const float s1 = rbf((__uint_as_float(p[0].z) + __uint_as_float(p[1].z)) + (__uint_as_float(p[2].z) + __uint_as_float(p[3].z)));
        const float o0 = __shfl_xor(s0, 32), o1 = __shfl_xor(s1, 32);
        if (lane < 32) hb[lane] = bfbits(s0 * o0) | (bfbits(s1 * o1) << 16);
    }
    __syncthreads();
    {
        const v2u hv = *(const v2u*)&hb[8 * rg + 2 * g];
        v4f o = {0.f, 0.f, 0.f, 0.f};
#pragma unroll
        for (int i = 0; i < 4; ++i) {
            const unsigned hw = hv[i >> 1];
            v4u bb;
#pragma unroll
            for (int jj = 0; jj < 4; ++jj) bb[jj] = __builtin_amdgcn_perm(hw, hw, (i & 1) ? sel1[jj] : sel0[jj]);
            o = mfma(dw[i], bb, o);
        }
        if (c < 8) *(v4f*)&red[w * 128 + 32 * g + 4 * c] = o;
    }
    __syncthreads();
    if (w < 2) {
        const int pos = w * 128 + 32 * (lane >> 4) + 8 * (lane & 3) + ((lane >> 2) & 3);
        float s0 = 0.f, s1 = 0.f;
#pragma unroll
        for (int k = 0; k < 4; ++k) { s0 += red[k * 256 + pos]; s1 += red[k * 256 + pos + 4]; }
        const v4u v = {__float_as_uint(s0), tag, __float_as_uint(s1), tag};
        const int j = 16 * q + 8 * w + (lane >> 3);
        __builtin_amdgcn_raw_buffer_store_b128(v, rs, X2_OFF + (j * 56 + 2 * cg + m) * 128 + (lane & 7) * 16, 0, AUX_SC1);
    } else if (w == 2 && b < 64) {
        const int vo = X2_OFF + b * (56 * 128) + lane * 16;
        v4u p[7];
        for (unsigned spin = 0; spin < SPIN_CAP; ++spin) {
#pragma unroll
            for (int j = 0; j < 7; ++j) p[j] = __builtin_amdgcn_raw_buffer_load_b128(rs, vo, j * 1024, AUX_SC1);
            bool ok = true;
#pragma unroll
            for (int j = 0; j < 7; ++j) ok &= (p[j].y == tag) & (p[j].w == tag);
            if (__all(ok)) break;
            __builtin_amdgcn_s_sleep(1);
        }
        float s0 = 0.f, s1 = 0.f;
#pragma unroll
        for (int j = 0; j < 7; ++j) { s0 += __uint_as_float(p[j].x); s1 += __uint_as_float(p[j].z); }
#pragma unroll
        for (int mm = 8; mm <= 32; mm <<= 1) { s0 += __shfl_xor(s0, mm); s1 += __shfl_xor(s1, mm); }
        if (lane < 8) ((GAS unsigned*)out)[b * 8 + lane] = bfbits(s0) | (bfbits(s1) << 16);
        if (b == 0 && lane == 0) __hip_atomic_store((GAS unsigned*)ws, tag, RLX_AGENT);
    }
}

extern "C" void kernel_launch(void* const* d_in, const int* in_sizes, int n_in,
                              void* d_out, int out_size, void* d_ws, size_t ws_size,
                              hipStream_t stream) {
    const unsigned short* x = (const unsigned short*)d_in[0];
    const unsigned short* wg = (const unsigned short*)d_in[1];
    const unsigned short* wu = (const unsigned short*)d_in[2];
    const unsigned short* wd = (const unsigned short*)d_in[3];
    k_fused<<<NWG, NT, 0, stream>>>(x, wg, wu, wd, (unsigned char*)d_ws, (unsigned short*)d_out);
}
```

```cpp
#define XF 0
#include <hip/hip_runtime.h>
#include <hip/hip_bf16.h>

#define GAS __attribute__((address_space(1)))
typedef unsigned v4u __attribute__((ext_vector_type(4)));
typedef unsigned v2u __attribute__((ext_vector_type(2)));
typedef float v4f __attribute__((ext_vector_type(4)));
typedef float v2f __attribute__((ext_vector_type(2)));
typedef __bf16 bf16x8 __attribute__((ext_vector_type(8)));
#define RLX_AGENT __ATOMIC_RELAXED, __HIP_MEMORY_SCOPE_AGENT

constexpr int D = 1024, F = 3584;
constexpr int NWG = 224, NT = 512;
constexpr unsigned TAGC = 0x9E3779B9u;
constexpr int X1_OFF = 4096;
constexpr int X2_OFF = X1_OFF + NWG * 1024;
constexpr int WS_BYTES = X2_OFF + 64 * 56 * 128;
constexpr int AUX_SC1 = 16, AUX_NT = 2;
constexpr unsigned SPIN_CAP = 1u << 16;

__device__ __forceinline__ v4f mfma(v4u a, v4u b, v4f c) {
    return __builtin_amdgcn_mfma_f32_16x16x32_bf16(__builtin_bit_cast(bf16x8, a), __builtin_bit_cast(bf16x8, b), c, 0, 0, 0);
}
__device__ __forceinline__ float rbf(float f) { return __bfloat162float(__float2bfloat16(f)); }
__device__ __forceinline__ unsigned bfbits(float f) { return (unsigned)__builtin_bit_cast(unsigned short, __float2bfloat16(f)); }

#define STAMP(k) do { if (XF == k || k == 0 || k == 6) { __builtin_amdgcn_sched_barrier(0); st[k] = __builtin_amdgcn_s_memrealtime(); __builtin_amdgcn_sched_barrier(0); } } while (0)
__global__ void __launch_bounds__(NT, 2) k_fused(const unsigned short* __restrict__ x, const unsigned short* __restrict__ wg,
                                              const unsigned short* __restrict__ wu, const unsigned short* __restrict__ wd,
                                              unsigned char* ws, unsigned short* __restrict__ out) {
    __shared__ __attribute__((aligned(16))) float red[8 * 128];
    __shared__ __attribute__((aligned(16))) unsigned hb[32];
    unsigned long long st[8] = {0,0,0,0,0,0,0,0}; STAMP(0);
    const int b = blockIdx.x, t = threadIdx.x, lane = t & 63;
    const int w = __builtin_amdgcn_readfirstlane(t >> 6);
    const int idx = (b & 7) * 28 + (b >> 3);
    const int cg = idx >> 3, sub = idx & 7, m = sub & 1, q = sub >> 1;
    const int g = lane >> 4, c = lane & 15;

    const __amdgpu_buffer_rsrc_t rw = __builtin_amdgcn_make_buffer_rsrc((void*)(m ? wu : wg), 0, D * F * 2, 0x00020000);
    const __amdgpu_buffer_rsrc_t rd = __builtin_amdgcn_make_buffer_rsrc((void*)wd, 0, D * F * 2, 0x00020000);
    const __amdgpu_buffer_rsrc_t rs = __builtin_amdgcn_make_buffer_rsrc((void*)ws, 0, WS_BYTES, 0x00020000);

    const int row0 = 256 * q + 8 * (4 * w + g);
    const v4u xv = *(const GAS v4u*)(x + row0);
    unsigned tag = __hip_atomic_load((GAS unsigned*)ws, RLX_AGENT) + TAGC;
    __builtin_amdgcn_sched_barrier(0);
    v4u a[8];
    {
        const int vo = row0 * (F * 2) + cg * 256 + c * 16;
#pragma unroll
        for (int i = 0; i < 8; ++i) a[i] = __builtin_amdgcn_raw_buffer_load_b128(rw, vo, i * (F * 2), AUX_NT);
    }
    __builtin_amdgcn_sched_barrier(0);
    __builtin_amdgcn_s_barrier();
    v4u dw[4];
    const int ch = w & 1, rg = w >> 1;
    {
        const int r = 128 * cg + 64 * m + 32 * (rg >> 1) + 4 * (rg & 1) + g;
        const int vo = r * (D * 2) + q * 512 + ch * 256 + c * 16;
#pragma unroll
        for (int i = 0; i < 4; ++i) dw[i] = __builtin_amdgcn_raw_buffer_load_b128(rd, vo, i * (8 * D * 2), AUX_NT);
    }
    __builtin_amdgcn_sched_barrier(0);
    STAMP(1);
    unsigned sel0[4], sel1[4];
#pragma unroll
    for (int jj = 0; jj < 4; ++jj) {
        const bool on = (c >> 1) == jj;
        sel0[jj] = on ? ((c & 1) ? 0x01000c0cu : 0x0c0c0100u) : 0x0c0c0c0cu;
        sel1[jj] = on ? ((c & 1) ? 0x03020c0cu : 0x0c0c0302u) : 0x0c0c0c0cu;
    }
    v4f acc = {0.f, 0.f, 0.f, 0.f};
#pragma unroll
    for (int i = 0; i < 8; ++i) {
        const unsigned xw = xv[i >> 1];
        v4u bb;
#pragma unroll
        for (int jj = 0; jj < 4; ++jj) bb[jj] = __builtin_amdgcn_perm(xw, xw, (i & 1) ? sel1[jj] : sel0[jj]);
        acc = mfma(a[i], bb, acc);
    }
    asm volatile("" : "+v"(acc)); STAMP(2);
    if (c < 8) *(v4f*)&red[w * 128 + 32 * g + 4 * c] = acc;
    asm volatile("" : "+v"(tag));
    __syncthreads();
    if (w == 0) {
        v2f s = *(const v2f*)&red[2 * lane];
#pragma unroll
        for (int k = 1; k < 8; ++k) s += *(const v2f*)&red[k * 128 + 2 * lane];
        const v4u v = {__float_as_uint(s.x), tag, __float_as_uint(s.y), tag};
        __builtin_amdgcn_raw_buffer_store_b128(v, rs, X1_OFF + idx * 1024 + lane * 16, 0, AUX_SC1);
        const int vo = X1_OFF + (cg * 8 + (lane >> 5)) * 1024 + m * 512 + (lane & 31) * 16;
        v4u p[4];
        for (unsigned spin = 0; spin < SPIN_CAP; ++spin) {
#pragma unroll
            for (int j = 0; j < 4; ++j) p[j] = __builtin_amdgcn_raw_buffer_load_b128(rs, vo, j * 2048, AUX_SC1);
            bool ok = true;
#pragma unroll
            for (int j = 0; j < 4; ++j) ok &= (p[j].y == tag) & (p[j].w == tag);
            if (__all(ok)) break;
            __builtin_amdgcn_s_sleep(1);
        }
        const float s0 = rbf((__uint_as_float(p[0].x) + __uint_as_float(p[1].x)) + (__uint_as_float(p[2].x) + __uint_as_float(p[3].x)));
        const float s1 = rbf((__uint_as_float(p[0].z) + __uint_as_float(p[1].z)) + (__uint_as_float(p[2].z) + __uint_as_float(p[3].z)));
        const float o0 = __shfl_xor(s0, 32), o1 = __shfl_xor(s1, 32);
        if (lane < 32) hb[lane] = bfbits(s0 * o0) | (bfbits(s1 * o1) << 16);
    }
    __syncthreads();
    {
        const v2u hv = *(const v2u*)&hb[8 * rg + 2 * g];
        v4f o = {0.f, 0.f, 0.f, 0.f};
#pragma unroll
        for (int i = 0; i < 4; ++i) {
            const unsigned hw = hv[i >> 1];
            v4u bb;
#pragma unroll
            for (int jj = 0; jj < 4; ++jj) bb[jj] = __builtin_amdgcn_perm(hw, hw, (i & 1) ? sel1[jj] : sel0[jj]);
            o = mfma(dw[i], bb, o);
        }
        if (c < 8) *(v4f*)&red[w * 128 + 32 * g + 4 * c] = o;
    }
    __syncthreads();
    STAMP(5);
    if (w < 2) {
        const int pos = w * 128 + 32 * (lane >> 4) + 8 * (lane & 3) + ((lane >> 2) & 3);
        float s0 = 0.f, s1 = 0.f;
#pragma unroll
        for (int k = 0; k < 4; ++k) { s0 += red[k * 256 + pos]; s1 += red[k * 256 + pos + 4]; }
        const v4u v = {__float_as_uint(s0), tag, __float_as_uint(s1), tag};
        const int j = 16 * q + 8 * w + (lane >> 3);
        __builtin_amdgcn_raw_buffer_store_b128(v, rs, X2_OFF + (j * 56 + 2 * cg + m) * 128 + (lane & 7) * 16, 0, AUX_SC1);
    } else if (w == 2 && b < 64) {
        const int vo = X2_OFF + b * (56 * 128) + lane * 16;
        v4u p[7];
        for (unsigned spin = 0; spin < SPIN_CAP; ++spin) {
#pragma unroll
            for (int j = 0; j < 7; ++j) p[j] = __builtin_amdgcn_raw_buffer_load_b128(rs, vo, j * 1024, AUX_SC1);
            bool ok = true;
#pragma unroll
            for (int j = 0; j < 7; ++j) ok &= (p[j].y == tag) & (p[j].w == tag);
            if (__all(ok)) break;
            __builtin_amdgcn_s_sleep(1);
        }
        float s0 = 0.f, s1 = 0.f;
#pragma unroll
        for (int j = 0; j < 7; ++j) { s0 += __uint_as_float(p[j].x); s1 += __uint_as_float(p[j].z); }
#pragma unroll
        for (int mm = 8; mm <= 32; mm <<= 1) { s0 += __shfl_xor(s0, mm); s1 += __shfl_xor(s1, mm); }
        if (lane < 8) ((GAS unsigned*)out)[b * 8 + lane] = bfbits(s0) | (bfbits(s1) << 16);
        if (b == 0 && lane == 0) __hip_atomic_store((GAS unsigned*)ws, tag, RLX_AGENT);
        STAMP(6);
        if (b == 0) {
            if (XF == 7) { const unsigned long long c0 = __builtin_amdgcn_s_memtime(); while (__builtin_amdgcn_s_memtime() - c0 < 48000ull) __builtin_amdgcn_s_sleep(1); }
            else if (XF > 0) { unsigned long long dt = st[XF] - st[0]; if (dt > 10000ull) dt = 10000ull; const unsigned long long until = st[6] + 8ull * dt; while (__builtin_amdgcn_s_memrealtime() < until) __builtin_amdgcn_s_sleep(1); }
        }
    }
}

extern "C" void kernel_launch(void* const* d_in, const int* in_sizes, int n_in,
                              void* d_out, int out_size, void* d_ws, size_t ws_size,
                              hipStream_t stream) {
    const unsigned short* x = (const unsigned short*)d_in[0];
    const unsigned short* wg = (const unsigned short*)d_in[1];
    const unsigned short* wu = (const unsigned short*)d_in[2];
    const unsigned short* wd = (const unsigned short*)d_in[3];
    k_fused<<<NWG, NT, 0, stream>>>(x, wg, wu, wd, (unsigned char*)d_ws, (unsigned short*)d_out);
}
```

```cpp
#include <hip/hip_runtime.h>

#define NPRE 4
#define P1DELAY 21
#define SLP1 0
#define SLP2 5
#define RDELAY 300
#define PREABS 0
#define OUTWARM 0
#define XLATE 2

#define STR_(x) #x
#define STR(x) STR_(x)

#define ACK_OFF 1024
#define X1_OFF 4096
#define X2_OFF 0x49000
#define WS_BYTES 0xb9000
#define W_BYTES 0x700000
#define TAGC 0x9E3779B9

#define STAMP(k)
#define STAMP2(k)
#define FIN "s_endpgm\n\t"
#define FIN2 FIN

#define WLD(i, so) "buffer_load_dwordx4 v[16+4*" STR(i) ":19+4*" STR(i) "], v2, s[24:27], " so " offen nt\n\t"
#define DLD_(i, so, off) "buffer_load_dwordx4 v[80+4*" STR(i) ":83+4*" STR(i) "], v3, s[28:31], " so " offen offset:" off " nt\n\t"
#define DLD0 DLD_(0, "0", "0")
#define DLD1 DLD_(1, "s61", "0")
#define DLD2 DLD_(2, "s62", "0")
#define DLD3 DLD_(3, "s63", "0")
#define DLD4 DLD_(4, "0", "2048")
#define DLD5 DLD_(5, "s61", "2048")
#define DLD6 DLD_(6, "s62", "2048")
#define DLD7 DLD_(7, "s63", "2048")
#define IFPRE(i) ".if " STR(i) " < " STR(NPRE) "\n\t" DLD##i ".endif\n\t"
#define IFPOST(i) ".if " STR(i) " >= " STR(NPRE) "\n\t" DLD##i ".endif\n\t"
#define MKB(i) "v_mov_b32 v7, s[36+2*" STR(i) "]\n\t" \
    "v_perm_b32 v[48+4*" STR(i) "], s[37+2*" STR(i) "], v7, v12\n\tv_perm_b32 v[49+4*" STR(i) "], s[37+2*" STR(i) "], v7, v13\n\t" \
    "v_perm_b32 v[50+4*" STR(i) "], s[37+2*" STR(i) "], v7, v14\n\tv_perm_b32 v[51+4*" STR(i) "], s[37+2*" STR(i) "], v7, v15\n\t"
#define MM1(i, c) "s_waitcnt vmcnt(7-" STR(i) "+" STR(NPRE) ")\n\t" \
    "v_mfma_f32_16x16x32_bf16 v[8:11], v[16+4*" STR(i) ":19+4*" STR(i) "], v[48+4*" STR(i) ":51+4*" STR(i) "], " c "\n\t"
#define MKH(i) "v_and_b32 v[24+4*(" STR(i) "&1)], v[16+" STR(i) "], v12\n\tv_and_b32 v[25+4*(" STR(i) "&1)], v[16+" STR(i) "], v13\n\t" \
    "v_and_b32 v[26+4*(" STR(i) "&1)], v[16+" STR(i) "], v14\n\tv_and_b32 v[27+4*(" STR(i) "&1)], v[16+" STR(i) "], v15\n\t"
#define MM2(i, c) "s_waitcnt vmcnt(7-" STR(i) ")\n\t" \
    "v_mfma_f32_16x16x32_bf16 v[8:11], v[80+4*" STR(i) ":83+4*" STR(i) "], v[24+4*(" STR(i) "&1):27+4*(" STR(i) "&1)], " c "\n\t"
#define TILESUM   \
    "ds_read2st64_b64 v[16:19], v5 offset1:1\n\tds_read2st64_b64 v[20:23], v5 offset0:2 offset1:3\n\t" \
    "ds_read2st64_b64 v[24:27], v5 offset0:4 offset1:5\n\tds_read2st64_b64 v[28:31], v5 offset0:6 offset1:7\n\t" \
    "s_waitcnt lgkmcnt(2)\n\t" \
    "v_add_f32 v16, v16, v18\n\tv_add_f32 v17, v17, v19\n\tv_add_f32 v20, v20, v22\n\tv_add_f32 v21, v21, v23\n\t" \
    "v_add_f32 v16, v16, v20\n\tv_add_f32 v17, v17, v21\n\t" \
    "s_waitcnt lgkmcnt(0)\n\t" \
    "v_add_f32 v24, v24, v26\n\tv_add_f32 v25, v25, v27\n\tv_add_f32 v28, v28, v30\n\tv_add_f32 v29, v29, v31\n\t" \
    "v_add_f32 v24, v24, v28\n\tv_add_f32 v25, v25, v29\n\t"
#define MKRS "s_mov_b32 s8, s12\n\ts_and_b32 s9, s13, 0xffff\n\ts_mov_b32 s10, " STR(WS_BYTES) "\n\ts_mov_b32 s11, 0x20000\n\t"
#define X3(d, r) "v_bitop3_b32 " d ", v[" STR(r) "+1], v[" STR(r) "+3], s52 bitop3:0x7e\n\t"
#define GLD4(r, sa, sb) \
    "buffer_load_dwordx4 v[" STR(r) ":" STR(r) "+3], v115, s[8:11], " sa " offen sc1\n\t" \
    "buffer_load_dwordx4 v[" STR(r) "+4:" STR(r) "+7], v115, s[8:11], " sa " offen offset:2048 sc1\n\t" \
    "buffer_load_dwordx4 v[" STR(r) "+8:" STR(r) "+11], v115, s[8:11], " sb " offen sc1\n\t" \
    "buffer_load_dwordx4 v[" STR(r) "+12:" STR(r) "+15], v115, s[8:11], " sb " offen offset:2048 sc1\n\t"
#define LD7(r, off) "global_load_dwordx4 v[" STR(r) ":" STR(r) "+3], v2, s[4:5] offset:" STR(off) " sc1\n\t"
#define ISS7(r) LD7(r, -3072) LD7(r+4, -2048) LD7(r+8, -1024) LD7(r+12, 0) LD7(r+16, 1024) LD7(r+20, 2048) LD7(r+24, 3072)
#define CHK7(r, lab) "s_waitcnt vmcnt(7)\n\t" X3("v8", r) X3("v9", r+4) X3("v10", r+8) "v_or3_b32 v8, v8, v9, v10\n\t" \
    X3("v9", r+12) X3("v10", r+16) "v_or3_b32 v8, v8, v9, v10\n\t" X3("v9", r+20) X3("v10", r+24) \
    "v_or3_b32 v8, v8, v9, v10\n\tv_cmp_ne_u32 vcc, 0, v8\n\ts_cbranch_vccz " lab "\n\t"
#define GATE(lab, t) lab ":\n\ts_memrealtime s[70:71]\n\ts_waitcnt lgkmcnt(0)\n\ts_sub_u32 s70, s70, s33\n\ts_cmpk_lt_u32 s70, " STR(t) "\n\ts_cbranch_scc1 " lab "\n\t"
#define AD(d, a, b) "v_add_f32 " d ", " a ", " b "\n\t"
#define VR(r) "v[" STR(r) "]"
#define SUM7(r) AD("v8", VR(r), VR(r+4)) AD("v10", VR(r+2), VR(r+6)) AD("v9", VR(r+8), VR(r+12)) AD("v11", VR(r+10), VR(r+14)) \
    AD("v8", "v8", VR(r+16)) AD("v10", "v10", VR(r+18)) AD("v9", "v9", VR(r+20)) AD("v11", "v11", VR(r+22)) \
    AD("v8", "v8", VR(r+24)) AD("v10", "v10", VR(r+26))

#define V10(a) "v" #a "0", "v" #a "1", "v" #a "2", "v" #a "3", "v" #a "4", "v" #a "5", "v" #a "6", "v" #a "7", "v" #a "8", "v" #a "9"
#define S10(a) "s" #a "0", "s" #a "1", "s" #a "2", "s" #a "3", "s" #a "4", "s" #a "5", "s" #a "6", "s" #a "7", "s" #a "8", "s" #a "9"

__global__ void __launch_bounds__(512, 4) __attribute__((aligned(4096)))
k_fused(const void* x, const void* wg, const void* wu, const void* wd, void* ws, void* out) {
    __shared__ __attribute__((aligned(16))) unsigned lds[1024 + 64 + 256];
    asm volatile(
        STAMP(0)
        "s_load_dwordx8 s[4:11], %[ka], 0x0\n\t"
        "s_load_dwordx4 s[12:15], %[ka], 0x20\n\t"
        "s_memrealtime s[62:63]\n\t"
        "v_lshrrev_b32 v6, 6, %[tid]\n\t"
        "v_and_b32 v1, 63, %[tid]\n\t"
        "v_readfirstlane_b32 s16, v6\n\t"
        "s_and_b32 s3, %[bid], 7\n\t"
        "s_lshr_b32 s17, %[bid], 3\n\t"
        "s_mov_b32 s26, " STR(W_BYTES) "\n\t"
        "s_and_b32 s19, s17, 3\n\t"
        "s_cmp_lg_u32 s19, 1\n\t"
        "s_cselect_b32 s21, 32, 24\n\t"
        "s_cselect_b32 s35, s26, 0\n\t"
        "s_mul_i32 s21, s21, s16\n\t"
        "s_lshl_b32 s18, s19, 3\n\t"
        "s_and_b32 s20, s16, 4\n\t"
        "s_add_u32 s18, s18, s20\n\t"
        "s_add_u32 s18, s18, 1\n\t"
        "s_and_b32 s20, s16, 3\n\t"
        "s_lshl_b32 s22, s20, 4\n\t"
        "s_addk_i32 s22, 192\n\t"
        "s_cmp_lt_u32 s17, 28\n\t"
        "s_cselect_b32 s18, s17, s18\n\t"
        "s_cselect_b32 s21, s21, s22\n\t"
        "s_cselect_b32 s34, s26, 0\n\t"
        "s_cselect_b32 s35, s35, 0\n\t"
        "s_cmp_lt_u32 s18, 28\n\t"
        "s_cselect_b32 s26, s26, 0\n\t"
        "s_lshr_b32 s22, s3, 1\n\t"
        "s_lshl_b32 s22, s22, 8\n\t"
        "s_add_u32 s21, s21, s22\n\t"
        "s_mul_i32 s22, s21, 7168\n\t"
        "s_lshl_b32 s23, s18, 8\n\t"
        "s_add_u32 s22, s22, s23\n\t"
        "s_lshl_b32 s21, s21, 1\n\t"
        "s_add_u32 s23, s21, 32\n\t"
        "s_min_u32 s23, s23, 2016\n\t"
        "v_lshrrev_b32 v6, 4, v1\n\t"
        "v_and_b32 v7, 15, v1\n\t"
        "v_mul_u32_u24 v2, 7168, v6\n\t"
        "v_lshl_add_u32 v2, v7, 4, v2\n\t"
        "v_add_u32 v2, s22, v2\n\t"
        "s_mov_b32 s27, 0x20000\n\t"
        "s_movk_i32 s54, 0x7000\n\t"
        "s_mov_b32 s55, 0xe000\n\ts_mov_b32 s56, 0x15000\n\ts_mov_b32 s57, 0x1c000\n\t"
        "s_mov_b32 s58, 0x23000\n\ts_mov_b32 s59, 0x2a000\n\ts_mov_b32 s60, 0x31000\n\t"
        "s_bitcmp0_b32 s3, 0\n\t"
        "s_waitcnt lgkmcnt(0)\n\t"
        STAMP2(7)
        "s_cselect_b32 s24, s6, s8\n\t"
        "s_cselect_b32 s25, s7, s9\n\t"
        "s_and_b32 s25, s25, 0xffff\n\t"
        WLD(0, "0")
        WLD(1, "s54") WLD(2, "s55") WLD(3, "s56")
        "s_load_dwordx8 s[36:43], s[4:5], s21\n\t"
        "s_load_dwordx8 s[44:51], s[4:5], s23\n\t"
        "s_load_dword s52, s[12:13], 0x0\n\t"
        "s_mov_b32 s26, s34\n\t" WLD(4, "s57") WLD(5, "s58")
        "s_mov_b32 s26, s35\n\t" WLD(6, "s59") WLD(7, "s60")
        "s_mov_b32 s33, s62\n\t"
        "s_mov_b32 s53, s16\n\t"
        "s_cmp_lg_u32 s3, 0\n\t"
        "s_cbranch_scc1 L_roles\n\t"
        "s_bitcmp1_b32 0xa0, s16\n\t"
        "s_cselect_b32 s53, 12, s53\n\t"
        "s_bitcmp1_b32 0x50, s16\n\t"
        "s_cselect_b32 s53, 1, s53\n\t"
        "s_add_u32 s21, %[bid], s16\n\t"
        "s_cmp_eq_u32 s21, 249\n\t"
        "s_cselect_b32 s53, 14, s53\n\t"
        "L_roles:\n\t"
        "s_mov_b32 s28, s10\n\t"
        "s_and_b32 s29, s11, 0xffff\n\t"
        "s_and_b32 s21, s53, 12\n\t"
        "s_cmp_eq_u32 s21, 4\n\t"
        "s_cselect_b32 s30, " STR(W_BYTES) ", 0\n\t"
        "s_mov_b32 s31, 0x20000\n\t"
        "s_lshl_b32 s18, s3, 3\n\t"
        "s_lshr_b32 s21, s17, 2\n\t"
        "s_add_u32 s18, s18, s21\n\t"
        "s_sub_u32 s18, s18, 8\n\t"
        "s_lshl_b32 s19, s19, 2\n\t"
        "s_add_u32 s19, s19, s20\n\t"
        "s_lshl_b32 s21, s18, 17\n\t"
        "s_lshl_b32 s22, s19, 7\n\t"
        "s_add_u32 s21, s21, s22\n\t"
        "v_and_b32 v3, 32, v1\n\t"
        "v_bfe_u32 v4, v1, 3, 2\n\t"
        "v_lshl_or_b32 v3, v4, 1, v3\n\t"
        "v_lshlrev_b32 v3, 11, v3\n\t"
        "v_and_b32 v4, 7, v1\n\t"
        "v_lshl_or_b32 v3, v4, 4, v3\n\t"
        "v_add_u32 v3, s21, v3\n\t"
        "s_movk_i32 s61, 0x4000\n\ts_mov_b32 s62, 0x8000\n\ts_mov_b32 s63, 0xc000\n\t"
        IFPRE(0) IFPRE(1) IFPRE(2) IFPRE(3) IFPRE(4) IFPRE(5) IFPRE(6) IFPRE(7)
        MKRS
        "v_mul_u32_u24 v8, 0x202, v6\n\t"
        "v_add_u32 v8, 0x100, v8\n\t"
        "v_and_b32 v9, 1, v1\n\t"
        "v_lshlrev_b32 v9, 4, v9\n\t"
        "v_xor_b32 v8, 0x0c0c, v8\n\t"
        "v_lshlrev_b32 v8, v9, v8\n\t"
        "v_mov_b32 v10, 0xffff\n\t"
        "v_lshlrev_b32 v112, v9, v10\n\t"
        "v_lshrrev_b32 v9, 1, v7\n\t"
        "v_mov_b32 v10, 0x0c0c0c0c\n\t"
        "v_xor_b32 v8, v8, v10\n\t"
        "v_cmp_eq_u32 s[22:23], 0, v9\n\tv_cmp_eq_u32 s[56:57], 1, v9\n\tv_cmp_eq_u32 s[58:59], 2, v9\n\tv_cmp_eq_u32 vcc, 3, v9\n\t"
        "v_cndmask_b32 v12, v10, v8, s[22:23]\n\tv_cndmask_b32 v13, v10, v8, s[56:57]\n\t"
        "v_cndmask_b32 v14, v10, v8, s[58:59]\n\tv_cndmask_b32 v15, v10, v8, vcc\n\t"
        "v_lshlrev_b32 v4, 4, v4\n\t"
        "v_lshl_add_u32 v4, v6, 7, v4\n\t"
        "s_lshl_b32 s21, s16, 9\n\t"
        "v_add_u32 v5, s21, v4\n\t"
        "v_add_u32 v4, 0x1100, v4\n\t"
        "v_cmp_gt_u32 vcc, 8, v7\n\t"
        "v_lshlrev_b32 v113, 4, v1\n\t"
        "s_lshl_b32 s21, %[bid], 10\n\t"
        "v_cndmask_b32 v4, v4, v5, vcc\n\t"
        "v_lshlrev_b32 v5, 3, v1\n\t"
        "s_addk_i32 s21, " STR(X1_OFF) "\n\t"
        "s_waitcnt lgkmcnt(0)\n\t"
        "s_add_u32 s52, s52, " STR(TAGC) "\n\t"
        MKB(0) MKB(1) MKB(2) MKB(3) MKB(4) MKB(5) MKB(6) MKB(7)
        "s_cmp_lg_u32 s53, 0\n\t"
        MM1(0, "0") MM1(1, "v[8:11]") MM1(2, "v[8:11]") MM1(3, "v[8:11]") MM1(4, "v[8:11]") MM1(5, "v[8:11]") MM1(6, "v[8:11]") MM1(7, "v[8:11]")
        "v_mov_b32 v61, s52\n\tv_mov_b32 v63, s52\n\t"
        "s_nop 5\n\t"
        "ds_write_b128 v4, v[8:11]\n\t"
        STAMP2(2)
        "s_waitcnt lgkmcnt(0)\n\t"
        "s_barrier\n\t"
        "s_cbranch_scc1 L_notpub\n\t"
        "s_cmp_lt_u32 s17, 28\n\t"
        TILESUM
        "s_cbranch_scc0 L_two\n\t"
        "v_add_f32 v60, v16, v24\n\tv_add_f32 v62, v17, v25\n\t"
        "L_store:\n\t"
        "buffer_store_dwordx4 v[60:63], v113, s[8:11], s21 offen sc1\n\t"
        "s_barrier\n\t"
        STAMP2(3)
        "s_cmp_eq_u32 s3, 0\n\t"
        "s_cbranch_scc1 L_fin0\n\t"
        "v_and_b32 v8, 31, v1\n\t"
        "v_lshrrev_b32 v9, 5, v1\n\t"
        "v_lshlrev_b32 v115, 4, v8\n\t"
        "v_lshl_add_u32 v115, v9, 10, v115\n\t"
        "v_lshlrev_b32 v116, 3, v8\n\t"
        "v_lshl_add_u32 v116, v9, 2, v116\n\t"
        "s_and_b32 s66, s18, 1\n\t"
        "s_lshl_b32 s66, s66, 9\n\t"
        "s_addk_i32 s66, " STR(X1_OFF) "\n\t"
        "s_lshr_b32 s64, s18, 1\n\t"
        "s_and_b32 s68, s64, 3\n\t"
        "s_lshl_b32 s64, s64, 13\n\t"
        "s_add_u32 s64, s64, s66\n\t"
        "s_add_u32 s65, s64, 4096\n\t"
        "s_lshr_b32 s67, s18, 3\n\t"
        "s_lshl_b32 s67, s67, 13\n\t"
        "s_add_u32 s66, s66, s67\n\t"
        "s_add_u32 s66, s66, 224*1024\n\t"
        "s_add_u32 s67, s66, 4096\n\t"
        "s_mov_b32 s69, 0x10000\n\t"
        "v_mov_b32 v32, 0\n\tv_mov_b32 v34, 0\n\tv_mov_b32 v6, 0\n\t"
        "s_sleep " STR(P1DELAY) "\n\t"
        "L_poll1:\n\t"
        "s_cmp_lg_u32 s68, 1\n\t"
        "s_cbranch_scc1 L_p1b\n\t"
        GLD4(32, "s66", "s67")
        "L_p1b:\n\t"
        GLD4(16, "s64", "s65")
        "s_cbranch_scc1 L_p1c\n\t"
        "s_waitcnt vmcnt(4)\n\t"
        X3("v8", 32) X3("v9", 36) X3("v10", 40) X3("v11", 44)
        "v_add_f32 v32, v32, v36\n\tv_add_f32 v34, v34, v38\n\tv_add_f32 v40, v40, v44\n\tv_add_f32 v42, v42, v46\n\t"
        "v_or3_b32 v6, v8, v9, v10\n\t"
        "v_add_f32 v32, v32, v40\n\tv_add_f32 v34, v34, v42\n\t"
        "v_or_b32 v6, v6, v11\n\t"
        "L_p1c:\n\t"
        "s_sub_u32 s69, s69, 1\n\t"
        "s_waitcnt vmcnt(2)\n\t"
        X3("v8", 16) X3("v9", 20)
        "v_add_f32 v16, v16, v20\n\tv_add_f32 v18, v18, v22\n\t"
        "v_or3_b32 v8, v8, v9, v6\n\t"
        "s_waitcnt vmcnt(0)\n\t"
        "v_add_f32 v24, v24, v28\n\tv_add_f32 v26, v26, v30\n\t"
        X3("v10", 24) X3("v11", 28)
        "v_add_f32 v16, v16, v24\n\tv_add_f32 v18, v18, v26\n\t"
        "v_add_f32 v16, v16, v32\n\tv_add_f32 v18, v18, v34\n\t"
        "v_or3_b32 v8, v8, v10, v11\n\t"
        "v_cmp_ne_u32 vcc, 0, v8\n\t"
        "v_permlane32_swap_b32 v16, v18\n\t"
        "s_cbranch_vccz L_got1\n\t"
        "s_sleep " STR(SLP1) "\n\t"
        "s_cbranch_scc0 L_poll1\n\t"
        "L_got1:\n\t"
        "v_cvt_pk_bf16_f32 v16, v16, v18\n\t"
        "v_lshlrev_b32 v18, 16, v16\n\t"
        "v_and_b32 v16, 0xffff0000, v16\n\t"
        "v_mul_f32 v16, v16, v18\n\t"
        "v_cvt_pk_bf16_f32 v16, v16, v16\n\t"
        "ds_write_b32 v116, v16 offset:4096\n\t"
        STAMP2(4)
        "s_waitcnt lgkmcnt(0)\n\t"
        "s_barrier\n\t"
        "L_fin0:\n\t"
        FIN2
        "L_two:\n\t"
        "s_sub_u32 s22, s17, 28\n\t"
        "s_lshl_b32 s22, s22, 13\n\t"
        "s_add_u32 s21, s21, s22\n\t"
        "v_mov_b32 v18, v17\n\tv_mov_b32 v17, s52\n\tv_mov_b32 v19, s52\n\t"
        "buffer_store_dwordx4 v[16:19], v113, s[8:11], s21 offen sc1\n\t"
        "s_addk_i32 s21, 8192\n\t"
        "v_mov_b32 v60, v24\n\tv_mov_b32 v62, v25\n\t"
        "s_branch L_store\n\t"
        "L_ack:\n\t"
        "s_lshl_b32 s21, %[bid], 3\n\t"
        "s_mov_b64 exec, 1\n\t"
        "buffer_store_dwordx2 v[62:63], off, s[8:11], s21 offset:" STR(ACK_OFF) " sc1\n\t"
        "L_end0:\n\t"
        "s_endpgm\n\t"
        "L_notpub:\n\t"
        "s_cmp_lt_u32 s53, 3\n\t"
        "s_cbranch_scc1 L_end0\n\t"
        "s_cmp_eq_u32 s53, 3\n\t"
        "s_barrier\n\t"
        "s_cbranch_scc1 L_ack\n\t"
        "s_cmp_gt_u32 s53, 7\n\t"
        "s_cbranch_scc1 L_notdown\n\t"
        IFPOST(0) IFPOST(1) IFPOST(2) IFPOST(3) IFPOST(4) IFPOST(5) IFPOST(6) IFPOST(7)
        "v_bfe_u32 v9, v1, 1, 2\n\t"
        "v_cmp_eq_u32 s[22:23], 0, v9\n\tv_cmp_eq_u32 s[56:57], 1, v9\n\tv_cmp_eq_u32 s[58:59], 2, v9\n\tv_cmp_eq_u32 vcc, 3, v9\n\t"
        "v_cndmask_b32 v12, 0, v112, s[22:23]\n\tv_cndmask_b32 v13, 0, v112, s[56:57]\n\t"
        "v_cndmask_b32 v14, 0, v112, s[58:59]\n\tv_cndmask_b32 v15, 0, v112, vcc\n\t"
        "v_and_b32 v5, 56, v1\n\t"
        "v_lshlrev_b32 v5, 2, v5\n\t"
        "s_mul_i32 s21, s19, 4*7168\n\t"
        "s_lshl_b32 s22, s18, 7\n\t"
        "s_add_u32 s21, s21, s22\n\t"
        "s_add_u32 s21, s21, " STR(X2_OFF) "\n\t"
        "v_mul_u32_u24 v4, 7168, v6\n\t"
        "v_and_b32 v7, 7, v1\n\t"
        "v_lshl_add_u32 v4, v7, 4, v4\n\t"
        "s_mov_b32 s22, 0x00ff00ff\n\ts_mov_b32 s23, 0x00ff00ff\n\t"
        "s_barrier\n\t"
        "ds_read_b128 v[16:19], v5 offset:4096\n\t"
        "ds_read_b128 v[20:23], v5 offset:4112\n\t"
        "s_waitcnt lgkmcnt(1)\n\t"
        MKH(0) MKH(1) MM2(0, "0") MKH(2) MM2(1, "v[8:11]") MKH(3) MM2(2, "v[8:11]") "s_waitcnt lgkmcnt(0)\n\t" STAMP2(4) MKH(4) MM2(3, "v[8:11]")
        MKH(5) MM2(4, "v[8:11]") MKH(6) MM2(5, "v[8:11]") MKH(7) MM2(6, "v[8:11]") "s_nop 0\n\t" MM2(7, "v[8:11]")
        "s_nop 7\n\t"
        "v_permlane32_swap_b32 v8, v9\n\t"
        "v_permlane32_swap_b32 v10, v11\n\t"
        "s_nop 1\n\t"
        "v_add_f32_dpp v60, v9, v8 row_ror:8 row_mask:0xf bank_mask:0xf\n\t"
        "v_add_f32_dpp v62, v11, v10 row_ror:8 row_mask:0xf bank_mask:0xf\n\t"
        "s_mov_b64 exec, s[22:23]\n\t"
        "buffer_store_dwordx4 v[60:63], v4, s[8:11], s21 offen sc1\n\t"
        STAMP2(5)
        FIN
        "L_notdown:\n\t"
        "s_mov_b32 s59, 0x10000\n\t"
        "s_cmp_eq_u32 s53, 14\n\t"
        "s_cbranch_scc1 L_poll3\n\t"
        "s_lshl_b32 s17, s17, 1\n\t"
        "s_bfe_u32 s18, s16, 0x10001\n\t"
        "s_add_u32 s17, s17, s18\n\t"
        "v_mov_b32 v2, v113\n\t"
        "s_mul_i32 s18, s17, 7168\n\t"
        "s_add_u32 s18, s18, " STR(X2_OFF) "+3072\n\t"
        "s_add_u32 s4, s12, s18\n\t"
        "s_addc_u32 s5, s13, 0\n\t"
        "s_lshr_b32 s18, s17, 2\n\ts_lshl_b32 s18, s18, 7\n\t"
        "s_and_b32 s19, s17, 1\n\ts_lshl_b32 s19, s19, 6\n\t"
        "s_and_b32 s20, s17, 2\n\ts_lshl_b32 s20, s20, 3\n\t"
        "s_add_u32 s18, s18, s19\n\ts_add_u32 s18, s18, s20\n\t"
        "s_add_u32 s14, s14, s18\n\ts_addc_u32 s15, s15, 0\n\t"
        "v_lshlrev_b32 v3, 1, v1\n\t"
        GATE("L_wait2", RDELAY)
        ISS7(16) "s_sleep " STR(SLP2) "\n\t" ISS7(44)
        "L_poll2:\n\t"
        CHK7(16, "L_got2a") ISS7(16) "s_sleep " STR(SLP2) "\n\t"
        CHK7(44, "L_got2b") ISS7(44) "s_sleep " STR(SLP2) "\n\t"
        "s_sub_u32 s59, s59, 1\n\t"
        "s_cbranch_scc0 L_poll2\n\t"
        "s_waitcnt vmcnt(0)\n\t"
        "L_got2a:\n\t" SUM7(16) "s_branch L_fin2\n\t"
        "L_got2b:\n\t" SUM7(44)
        "L_fin2:\n\t"
        STAMP2(5)
        "v_add_f32 v8, v8, v9\n\tv_add_f32 v10, v10, v11\n\t"
        "s_nop 1\n\t"
        "v_add_f32_dpp v8, v8, v8 row_ror:8 row_mask:0xf bank_mask:0xf\n\t"
        "v_add_f32_dpp v10, v10, v10 row_ror:8 row_mask:0xf bank_mask:0xf\n\t"
        "s_nop 1\n\t"
        "v_permlane16_swap_b32 v8, v10\n\t"
        "s_nop 0\n\t"
        "v_add_f32 v8, v8, v10\n\t"
        "v_mov_b32 v9, v8\n\t"
        "s_nop 1\n\t"
        "v_permlane32_swap_b32 v8, v9\n\t"
        "s_nop 0\n\t"
        "v_add_f32 v8, v8, v9\n\t"
        "v_cvt_pk_bf16_f32 v8, v8, v8\n\t"
        "s_mov_b64 exec, 0x00ff00ff\n\t"
        "global_store_short v3, v8, s[14:15]\n\t"
        STAMP(6)
        FIN
        "L_poll3:\n\t"
        "s_sleep 16\n\t"
        "buffer_load_dwordx4 v[16:19], v113, s[8:11], 0 offen offset:" STR(ACK_OFF) " sc1\n\t"
        "buffer_load_dwordx4 v[20:23], v113, s[8:11], 0 offen offset:" STR(ACK_OFF) "+1024 sc1\n\t"
        "s_sub_u32 s59, s59, 1\n\t"
        "s_waitcnt vmcnt(0)\n\t"
        X3("v8", 16) X3("v9", 20)
        "v_or_b32 v8, v8, v9\n\t"
        "v_cmp_ne_u32 vcc, 0, v8\n\t"
        "s_cbranch_vccz L_got3\n\t"
        "s_cbranch_scc0 L_poll3\n\t"
        "L_got3:\n\t"
        "s_mov_b64 exec, 1\n\t"
        "buffer_store_dword v63, off, s[8:11], 0 sc1\n\t"
        "s_endpgm\n\t"
        :
        : [ka] "s"(__builtin_amdgcn_kernarg_segment_ptr()), [bid] "s"(blockIdx.x), [tid] "v"(threadIdx.x), [lds] "v"((__attribute__((address_space(3))) unsigned*)lds)
        : "memory", "vcc", "scc",
          "v1", "v2", "v3", "v4", "v5", "v6", "v7", "v8", "v9", V10(1), V10(2), V10(3), V10(4), V10(5), V10(6), V10(7), V10(8), V10(9), V10(10),
          "v110", "v111", "v112", "v113", "v114", "v115", "v116",
          "s3", "s4", "s5", "s6", "s7", "s8", "s9", S10(1), S10(2), "s30", "s31", "s33", "s34", "s35", "s36", "s37", "s38", "s39", S10(4), S10(5), S10(6), "s70", "s71"
          );
}

extern "C" void kernel_launch(void* const* d_in, const int* in_sizes, int n_in,
                              void* d_out, int out_size, void* d_ws, size_t ws_size,
                              hipStream_t stream) {
    k_fused<<<256, 512, 0, stream>>>(d_in[0], d_in[1], d_in[2], d_in[3], d_ws, d_out);
}
```

```cpp
#include <hip/hip_runtime.h>

#define NPRE 4
#define P1DELAY 21
#define SLP2 4
#define RDELAY 290

#define STR_(x) #x
#define STR(x) STR_(x)

#define ACK_OFF 1024
#define X1_OFF 4096
#define X2_OFF 0x49000
#define WS_BYTES 0xb9000
#define W_BYTES 0x700000
#define TAGC 0x9E3779B9

#define WLD(i, so) "buffer_load_dwordx4 v[16+4*" STR(i) ":19+4*" STR(i) "], v2, s[24:27], " so " offen nt\n\t"
#define DLD_(i, so, off) "buffer_load_dwordx4 v[80+4*" STR(i) ":83+4*" STR(i) "], v3, s[28:31], " so " offen offset:" off " nt\n\t"
#define DLD0 DLD_(0, "0", "0")
#define DLD1 DLD_(1, "s61", "0")
#define DLD2 DLD_(2, "s62", "0")
#define DLD3 DLD_(3, "s63", "0")
#define DLD4 DLD_(4, "0", "2048")
#define DLD5 DLD_(5, "s61", "2048")
#define DLD6 DLD_(6, "s62", "2048")
#define DLD7 DLD_(7, "s63", "2048")
#define IFPRE(i) ".if " STR(i) " < " STR(NPRE) "\n\t" DLD##i ".endif\n\t"
#define IFPOST(i) ".if " STR(i) " >= " STR(NPRE) "\n\t" DLD##i ".endif\n\t"
#define MKB(i) "v_mov_b32 v7, s[36+2*" STR(i) "]\n\t" \
    "v_perm_b32 v[48+4*" STR(i) "], s[37+2*" STR(i) "], v7, v12\n\tv_perm_b32 v[49+4*" STR(i) "], s[37+2*" STR(i) "], v7, v13\n\t" \
    "v_perm_b32 v[50+4*" STR(i) "], s[37+2*" STR(i) "], v7, v14\n\tv_perm_b32 v[51+4*" STR(i) "], s[37+2*" STR(i) "], v7, v15\n\t"
#define MM1(i, c) "s_waitcnt vmcnt(7-" STR(i) "+" STR(NPRE) ")\n\t" \
    "v_mfma_f32_16x16x32_bf16 v[8:11], v[16+4*" STR(i) ":19+4*" STR(i) "], v[48+4*" STR(i) ":51+4*" STR(i) "], " c "\n\t"
#define MKH(i) "v_and_b32 v[24+4*(" STR(i) "&1)], v[16+" STR(i) "], v12\n\tv_and_b32 v[25+4*(" STR(i) "&1)], v[16+" STR(i) "], v13\n\t" \
    "v_and_b32 v[26+4*(" STR(i) "&1)], v[16+" STR(i) "], v14\n\tv_and_b32 v[27+4*(" STR(i) "&1)], v[16+" STR(i) "], v15\n\t"
#define MM2(i, c) "s_waitcnt vmcnt(7-" STR(i) ")\n\t" \
    "v_mfma_f32_16x16x32_bf16 v[8:11], v[80+4*" STR(i) ":83+4*" STR(i) "], v[24+4*(" STR(i) "&1):27+4*(" STR(i) "&1)], " c "\n\t"
#define X3(d, r) "v_bitop3_b32 " d ", v[" STR(r) "+1], v[" STR(r) "+3], s52 bitop3:0x7e\n\t"
#define GLD4(r, sa, sb) \
    "buffer_load_dwordx4 v[" STR(r) ":" STR(r) "+3], v115, s[8:11], " sa " offen sc1\n\t" \
    "buffer_load_dwordx4 v[" STR(r) "+4:" STR(r) "+7], v115, s[8:11], " sa " offen offset:2048 sc1\n\t" \
    "buffer_load_dwordx4 v[" STR(r) "+8:" STR(r) "+11], v115, s[8:11], " sb " offen sc1\n\t" \
    "buffer_load_dwordx4 v[" STR(r) "+12:" STR(r) "+15], v115, s[8:11], " sb " offen offset:2048 sc1\n\t"
#define LD7(r, off) "global_load_dwordx4 v[" STR(r) ":" STR(r) "+3], v113, s[4:5] offset:" STR(off) " sc1\n\t"
#define ISS7(r) LD7(r, -3072) LD7(r+4, -2048) LD7(r+8, -1024) LD7(r+12, 0) LD7(r+16, 1024) LD7(r+20, 2048) LD7(r+24, 3072)
#define CHK7(r, lab) "s_waitcnt vmcnt(7)\n\t" X3("v8", r) X3("v9", r+4) X3("v10", r+8) "v_or3_b32 v8, v8, v9, v10\n\t" \
    X3("v9", r+12) X3("v10", r+16) "v_or3_b32 v8, v8, v9, v10\n\t" X3("v9", r+20) X3("v10", r+24) \
    "v_or3_b32 v8, v8, v9, v10\n\tv_cmp_ne_u32 vcc, 0, v8\n\ts_cbranch_vccz " lab "\n\t"
#define AD(d, a, b) "v_add_f32 " d ", " a ", " b "\n\t"
#define VR(r) "v[" STR(r) "]"
#define SUM7(r) AD("v8", VR(r), VR(r+4)) AD("v10", VR(r+2), VR(r+6)) AD("v9", VR(r+8), VR(r+12)) AD("v11", VR(r+10), VR(r+14)) \
    AD("v8", "v8", VR(r+16)) AD("v10", "v10", VR(r+18)) AD("v9", "v9", VR(r+20)) AD("v11", "v11", VR(r+22)) \
    AD("v8", "v8", VR(r+24)) AD("v10", "v10", VR(r+26))

#define V10(a) "v" #a "0", "v" #a "1", "v" #a "2", "v" #a "3", "v" #a "4", "v" #a "5", "v" #a "6", "v" #a "7", "v" #a "8", "v" #a "9"
#define S10(a) "s" #a "0", "s" #a "1", "s" #a "2", "s" #a "3", "s" #a "4", "s" #a "5", "s" #a "6", "s" #a "7", "s" #a "8", "s" #a "9"

__global__ void __launch_bounds__(512, 4) __attribute__((aligned(4096)))
glu_bilinear_decode(const void* x, const void* wg, const void* wu, const void* wd, void* ws, void* out) {
    __shared__ __attribute__((aligned(16))) unsigned lds[1024 + 64 + 256];
    asm volatile(
        "s_load_dwordx8 s[4:11], %[ka], 0x0\n\t"
        "s_load_dwordx4 s[12:15], %[ka], 0x20\n\t"
        "s_memrealtime s[62:63]\n\t"
        "v_lshrrev_b32 v6, 6, %[tid]\n\t"
        "v_and_b32 v1, 63, %[tid]\n\t"
        "v_readfirstlane_b32 s16, v6\n\t"
        "s_and_b32 s3, %[bid], 7\n\t"
        "s_lshr_b32 s17, %[bid], 3\n\t"
        "s_mov_b32 s26, " STR(W_BYTES) "\n\t"
        "s_and_b32 s19, s17, 3\n\t"
        "s_cmp_lg_u32 s19, 1\n\t"
        "s_cselect_b32 s21, 32, 24\n\t"
        "s_cselect_b32 s35, s26, 0\n\t"
        "s_mul_i32 s21, s21, s16\n\t"
        "s_lshl_b32 s18, s19, 3\n\t"
        "s_and_b32 s20, s16, 4\n\t"
        "s_add_u32 s18, s18, s20\n\t"
        "s_add_u32 s18, s18, 1\n\t"
        "s_and_b32 s20, s16, 3\n\t"
        "s_lshl_b32 s22, s20, 4\n\t"
        "s_addk_i32 s22, 192\n\t"
        "s_cmp_lt_u32 s17, 28\n\t"
        "s_cselect_b32 s18, s17, s18\n\t"
        "s_cselect_b32 s21, s21, s22\n\t"
        "s_cselect_b32 s34, s26, 0\n\t"
        "s_cselect_b32 s35, s35, 0\n\t"
        "s_cmp_lt_u32 s18, 28\n\t"
        "s_cselect_b32 s26, s26, 0\n\t"
        "s_lshr_b32 s22, s3, 1\n\t"
        "s_lshl_b32 s22, s22, 8\n\t"
        "s_add_u32 s21, s21, s22\n\t"
        "s_mul_i32 s22, s21, 7168\n\t"
        "s_lshl_b32 s23, s18, 8\n\t"
        "s_add_u32 s22, s22, s23\n\t"
        "s_lshl_b32 s21, s21, 1\n\t"
        "s_add_u32 s23, s21, 32\n\t"
        "s_min_u32 s23, s23, 2016\n\t"
        "v_lshrrev_b32 v6, 4, v1\n\t"
        "v_and_b32 v7, 15, v1\n\t"
        "v_mul_u32_u24 v2, 7168, v6\n\t"
        "v_lshl_add_u32 v2, v7, 4, v2\n\t"
        "v_add_u32 v2, s22, v2\n\t"
        "s_mov_b32 s27, 0x20000\n\t"
        "s_movk_i32 s54, 0x7000\n\t"
        "s_mov_b32 s55, 0xe000\n\ts_mov_b32 s56, 0x15000\n\ts_mov_b32 s57, 0x1c000\n\t"
        "s_mov_b32 s58, 0x23000\n\ts_mov_b32 s59, 0x2a000\n\ts_mov_b32 s60, 0x31000\n\t"
        "s_bitcmp0_b32 s3, 0\n\t"
        "s_waitcnt lgkmcnt(0)\n\t"
        "s_cselect_b32 s24, s6, s8\n\t"
        "s_cselect_b32 s25, s7, s9\n\t"
        "s_and_b32 s25, s25, 0xffff\n\t"
        WLD(0, "0") WLD(1, "s54") WLD(2, "s55") WLD(3, "s56")
        "s_load_dwordx8 s[36:43], s[4:5], s21\n\t"
        "s_load_dwordx8 s[44:51], s[4:5], s23\n\t"
        "s_load_dword s52, s[12:13], 0x0\n\t"
        "s_mov_b32 s26, s34\n\t" WLD(4, "s57") WLD(5, "s58")
        "s_mov_b32 s26, s35\n\t" WLD(6, "s59") WLD(7, "s60")
        "s_mov_b32 s33, s62\n\t"
        "s_mov_b32 s53, s16\n\t"
        "s_cmp_lg_u32 s3, 0\n\t"
        "s_cbranch_scc1 L_roles\n\t"
        "s_bitcmp1_b32 0xa0, s16\n\t"
        "s_cselect_b32 s53, 12, s53\n\t"
        "s_bitcmp1_b32 0x50, s16\n\t"
        "s_cselect_b32 s53, 1, s53\n\t"
        "s_add_u32 s21, %[bid], s16\n\t"
        "s_cmp_eq_u32 s21, 249\n\t"
        "s_cselect_b32 s53, 14, s53\n\t"
        "L_roles:\n\t"
        "s_mov_b32 s28, s10\n\t"
        "s_and_b32 s29, s11, 0xffff\n\t"
        "s_and_b32 s21, s53, 12\n\t"
        "s_cmp_eq_u32 s21, 4\n\t"
        "s_cselect_b32 s30, " STR(W_BYTES) ", 0\n\t"
        "s_mov_b32 s31, 0x20000\n\t"
        "s_lshl_b32 s18, s3, 3\n\t"
        "s_lshr_b32 s21, s17, 2\n\t"
        "s_add_u32 s18, s18, s21\n\t"
        "s_sub_u32 s18, s18, 8\n\t"
        "s_lshl_b32 s19, s19, 2\n\t"
        "s_add_u32 s19, s19, s20\n\t"
        "s_lshl_b32 s21, s18, 17\n\t"
        "s_lshl_b32 s22, s19, 7\n\t"
        "s_add_u32 s21, s21, s22\n\t"
        "v_and_b32 v3, 32, v1\n\t"
        "v_bfe_u32 v4, v1, 3, 2\n\t"
        "v_lshl_or_b32 v3, v4, 1, v3\n\t"
        "v_lshlrev_b32 v3, 11, v3\n\t"
        "v_and_b32 v4, 7, v1\n\t"
        "v_lshl_or_b32 v3, v4, 4, v3\n\t"
        "v_add_u32 v3, s21, v3\n\t"
        "s_movk_i32 s61, 0x4000\n\ts_mov_b32 s62, 0x8000\n\ts_mov_b32 s63, 0xc000\n\t"
        IFPRE(0) IFPRE(1) IFPRE(2) IFPRE(3) IFPRE(4) IFPRE(5) IFPRE(6) IFPRE(7)
        "s_mov_b32 s8, s12\n\ts_and_b32 s9, s13, 0xffff\n\ts_mov_b32 s10, " STR(WS_BYTES) "\n\ts_mov_b32 s11, 0x20000\n\t"
        "v_mul_u32_u24 v8, 0x202, v6\n\t"
        "v_add_u32 v8, 0x100, v8\n\t"
        "v_and_b32 v9, 1, v1\n\t"
        "v_lshlrev_b32 v9, 4, v9\n\t"
        "v_xor_b32 v8, 0x0c0c, v8\n\t"
        "v_lshlrev_b32 v8, v9, v8\n\t"
        "v_mov_b32 v10, 0xffff\n\t"
        "v_lshlrev_b32 v112, v9, v10\n\t"
        "v_lshrrev_b32 v9, 1, v7\n\t"
        "v_mov_b32 v10, 0x0c0c0c0c\n\t"
        "v_xor_b32 v8, v8, v10\n\t"
        "v_cmp_eq_u32 s[22:23], 0, v9\n\tv_cmp_eq_u32 s[56:57], 1, v9\n\tv_cmp_eq_u32 s[58:59], 2, v9\n\tv_cmp_eq_u32 vcc, 3, v9\n\t"
        "v_cndmask_b32 v12, v10, v8, s[22:23]\n\tv_cndmask_b32 v13, v10, v8, s[56:57]\n\t"
        "v_cndmask_b32 v14, v10, v8, s[58:59]\n\tv_cndmask_b32 v15, v10, v8, vcc\n\t"
        "v_lshlrev_b32 v4, 4, v4\n\t"
        "v_lshl_add_u32 v4, v6, 7, v4\n\t"
        "s_lshl_b32 s21, s16, 9\n\t"
        "v_add_u32 v5, s21, v4\n\t"
        "v_add_u32 v4, 0x1100, v4\n\t"
        "v_cmp_gt_u32 vcc, 8, v7\n\t"
        "v_lshlrev_b32 v113, 4, v1\n\t"
        "s_lshl_b32 s21, %[bid], 10\n\t"
        "v_cndmask_b32 v4, v4, v5, vcc\n\t"
        "v_lshlrev_b32 v5, 3, v1\n\t"
        "s_addk_i32 s21, " STR(X1_OFF) "\n\t"
        "s_waitcnt lgkmcnt(0)\n\t"
        "s_add_u32 s52, s52, " STR(TAGC) "\n\t"
        MKB(0) MKB(1) MKB(2) MKB(3) MKB(4) MKB(5) MKB(6) MKB(7)
        "s_cmp_lg_u32 s53, 0\n\t"
        MM1(0, "0") MM1(1, "v[8:11]") MM1(2, "v[8:11]") MM1(3, "v[8:11]") MM1(4, "v[8:11]") MM1(5, "v[8:11]") MM1(6, "v[8:11]") MM1(7, "v[8:11]")
        "v_mov_b32 v61, s52\n\tv_mov_b32 v63, s52\n\t"
        "s_nop 5\n\t"
        "ds_write_b128 v4, v[8:11]\n\t"
        "s_waitcnt lgkmcnt(0)\n\t"
        "s_barrier\n\t"
        "s_cbranch_scc1 L_notpub\n\t"
        "s_cmp_lt_u32 s17, 28\n\t"
        "ds_read2st64_b64 v[16:19], v5 offset1:1\n\tds_read2st64_b64 v[20:23], v5 offset0:2 offset1:3\n\t"
        "ds_read2st64_b64 v[24:27], v5 offset0:4 offset1:5\n\tds_read2st64_b64 v[28:31], v5 offset0:6 offset1:7\n\t"
        "s_waitcnt lgkmcnt(2)\n\t"
        "v_add_f32 v16, v16, v18\n\tv_add_f32 v17, v17, v19\n\tv_add_f32 v20, v20, v22\n\tv_add_f32 v21, v21, v23\n\t"
        "v_add_f32 v16, v16, v20\n\tv_add_f32 v17, v17, v21\n\t"
        "s_waitcnt lgkmcnt(0)\n\t"
        "v_add_f32 v24, v24, v26\n\tv_add_f32 v25, v25, v27\n\tv_add_f32 v28, v28, v30\n\tv_add_f32 v29, v29, v31\n\t"
        "v_add_f32 v24, v24, v28\n\tv_add_f32 v25, v25, v29\n\t"
        "s_cbranch_scc0 L_two\n\t"
        "v_add_f32 v60, v16, v24\n\tv_add_f32 v62, v17, v25\n\t"
        "L_store:\n\t"
        "buffer_store_dwordx4 v[60:63], v113, s[8:11], s21 offen sc1\n\t"
        "s_barrier\n\t"
        "s_cmp_eq_u32 s3, 0\n\t"
        "s_cbranch_scc1 L_end0\n\t"
        "v_and_b32 v8, 31, v1\n\t"
        "v_lshrrev_b32 v9, 5, v1\n\t"
        "v_lshlrev_b32 v115, 4, v8\n\t"
        "v_lshl_add_u32 v115, v9, 10, v115\n\t"
        "v_lshlrev_b32 v114, 3, v8\n\t"
        "v_lshl_add_u32 v114, v9, 2, v114\n\t"
        "s_and_b32 s66, s18, 1\n\t"
        "s_lshl_b32 s66, s66, 9\n\t"
        "s_addk_i32 s66, " STR(X1_OFF) "\n\t"
        "s_lshr_b32 s64, s18, 1\n\t"
        "s_and_b32 s68, s64, 3\n\t"
        "s_lshl_b32 s64, s64, 13\n\t"
        "s_add_u32 s64, s64, s66\n\t"
        "s_add_u32 s65, s64, 4096\n\t"
        "s_lshr_b32 s67, s18, 3\n\t"
        "s_lshl_b32 s67, s67, 13\n\t"
        "s_add_u32 s66, s66, s67\n\t"
        "s_add_u32 s66, s66, 224*1024\n\t"
        "s_add_u32 s67, s66, 4096\n\t"
        "s_mov_b32 s69, 0x10000\n\t"
        "v_mov_b32 v32, 0\n\tv_mov_b32 v34, 0\n\tv_mov_b32 v6, 0\n\t"
        "s_sleep " STR(P1DELAY) "\n\t"
        "L_poll1:\n\t"
        "s_cmp_lg_u32 s68, 1\n\t"
        "s_cbranch_scc1 L_p1b\n\t"
        GLD4(32, "s66", "s67")
        "L_p1b:\n\t"
        GLD4(16, "s64", "s65")
        "s_cbranch_scc1 L_p1c\n\t"
        "s_waitcnt vmcnt(4)\n\t"
        X3("v8", 32) X3("v9", 36) X3("v10", 40) X3("v11", 44)
        "v_add_f32 v32, v32, v36\n\tv_add_f32 v34, v34, v38\n\tv_add_f32 v40, v40, v44\n\tv_add_f32 v42, v42, v46\n\t"
        "v_or3_b32 v6, v8, v9, v10\n\t"
        "v_add_f32 v32, v32, v40\n\tv_add_f32 v34, v34, v42\n\t"
        "v_or_b32 v6, v6, v11\n\t"
        "L_p1c:\n\t"
        "s_sub_u32 s69, s69, 1\n\t"
        "s_waitcnt vmcnt(2)\n\t"
        X3("v8", 16) X3("v9", 20)
        "v_add_f32 v16, v16, v20\n\tv_add_f32 v18, v18, v22\n\t"
        "v_or3_b32 v8, v8, v9, v6\n\t"
        "s_waitcnt vmcnt(0)\n\t"
        "v_add_f32 v24, v24, v28\n\tv_add_f32 v26, v26, v30\n\t"
        X3("v10", 24) X3("v11", 28)
        "v_add_f32 v16, v16, v24\n\tv_add_f32 v18, v18, v26\n\t"
        "v_add_f32 v16, v16, v32\n\tv_add_f32 v18, v18, v34\n\t"
        "v_or3_b32 v8, v8, v10, v11\n\t"
        "v_cmp_ne_u32 vcc, 0, v8\n\t"
        "v_permlane32_swap_b32 v16, v18\n\t"
        "s_cbranch_vccz L_got1\n\t"
        "s_cbranch_scc0 L_poll1\n\t"
        "L_got1:\n\t"
        "v_cvt_pk_bf16_f32 v16, v16, v18\n\t"
        "v_lshlrev_b32 v18, 16, v16\n\t"
        "v_and_b32 v16, 0xffff0000, v16\n\t"
        "v_mul_f32 v16, v16, v18\n\t"
        "v_cvt_pk_bf16_f32 v16, v16, v16\n\t"
        "ds_write_b32 v114, v16 offset:4096\n\t"
        "s_waitcnt lgkmcnt(0)\n\t"
        "s_barrier\n\t"
        "s_endpgm\n\t"
        "L_two:\n\t"
        "s_sub_u32 s22, s17, 28\n\t"
        "s_lshl_b32 s22, s22, 13\n\t"
        "s_add_u32 s21, s21, s22\n\t"
        "v_mov_b32 v18, v17\n\tv_mov_b32 v17, s52\n\tv_mov_b32 v19, s52\n\t"
        "buffer_store_dwordx4 v[16:19], v113, s[8:11], s21 offen sc1\n\t"
        "s_addk_i32 s21, 8192\n\t"
        "v_mov_b32 v60, v24\n\tv_mov_b32 v62, v25\n\t"
        "s_branch L_store\n\t"
        "L_ack:\n\t"
        "s_lshl_b32 s21, %[bid], 3\n\t"
        "s_mov_b64 exec, 1\n\t"
        "buffer_store_dwordx2 v[62:63], off, s[8:11], s21 offset:" STR(ACK_OFF) " sc1\n\t"
        "L_end0:\n\t"
        "s_endpgm\n\t"
        "L_notpub:\n\t"
        "s_cmp_lt_u32 s53, 3\n\t"
        "s_cbranch_scc1 L_end0\n\t"
        "s_cmp_eq_u32 s53, 3\n\t"
        "s_barrier\n\t"
        "s_cbranch_scc1 L_ack\n\t"
        "s_cmp_gt_u32 s53, 7\n\t"
        "s_cbranch_scc1 L_notdown\n\t"
        IFPOST(0) IFPOST(1) IFPOST(2) IFPOST(3) IFPOST(4) IFPOST(5) IFPOST(6) IFPOST(7)
        "v_bfe_u32 v9, v1, 1, 2\n\t"
        "v_cmp_eq_u32 s[22:23], 0, v9\n\tv_cmp_eq_u32 s[56:57], 1, v9\n\tv_cmp_eq_u32 s[58:59], 2, v9\n\tv_cmp_eq_u32 vcc, 3, v9\n\t"
        "v_cndmask_b32 v12, 0, v112, s[22:23]\n\tv_cndmask_b32 v13, 0, v112, s[56:57]\n\t"
        "v_cndmask_b32 v14, 0, v112, s[58:59]\n\tv_cndmask_b32 v15, 0, v112, vcc\n\t"
        "v_and_b32 v5, 56, v1\n\t"
        "v_lshlrev_b32 v5, 2, v5\n\t"
        "s_mul_i32 s21, s19, 4*7168\n\t"
        "s_lshl_b32 s22, s18, 7\n\t"
        "s_add_u32 s21, s21, s22\n\t"
        "s_add_u32 s21, s21, " STR(X2_OFF) "\n\t"
        "v_mul_u32_u24 v4, 7168, v6\n\t"
        "v_and_b32 v7, 7, v1\n\t"
        "v_lshl_add_u32 v4, v7, 4, v4\n\t"
        "s_mov_b32 s22, 0x00ff00ff\n\ts_mov_b32 s23, 0x00ff00ff\n\t"
        "s_barrier\n\t"
        "ds_read_b128 v[16:19], v5 offset:4096\n\t"
        "ds_read_b128 v[20:23], v5 offset:4112\n\t"
        "s_waitcnt lgkmcnt(1)\n\t"
        MKH(0) MKH(1) MM2(0, "0") MKH(2) MM2(1, "v[8:11]") MKH(3) MM2(2, "v[8:11]") "s_waitcnt lgkmcnt(0)\n\t" MKH(4) MM2(3, "v[8:11]")
        MKH(5) MM2(4, "v[8:11]") MKH(6) MM2(5, "v[8:11]") MKH(7) MM2(6, "v[8:11]") "s_nop 0\n\t" MM2(7, "v[8:11]")
        "s_nop 7\n\t"
        "v_permlane32_swap_b32 v8, v9\n\t"
        "v_permlane32_swap_b32 v10, v11\n\t"
        "s_nop 1\n\t"
        "v_add_f32_dpp v60, v9, v8 row_ror:8 row_mask:0xf bank_mask:0xf\n\t"
        "v_add_f32_dpp v62, v11, v10 row_ror:8 row_mask:0xf bank_mask:0xf\n\t"
        "s_mov_b64 exec, s[22:23]\n\t"
        "buffer_store_dwordx4 v[60:63], v4, s[8:11], s21 offen sc1\n\t"
        "s_endpgm\n\t"
        "L_notdown:\n\t"
        "s_mov_b32 s59, 0x10000\n\t"
        "s_cmp_eq_u32 s53, 14\n\t"
        "s_cbranch_scc1 L_poll3\n\t"
        "s_lshl_b32 s17, s17, 1\n\t"
        "s_bfe_u32 s18, s16, 0x10001\n\t"
        "s_add_u32 s17, s17, s18\n\t"
        "s_mul_i32 s18, s17, 7168\n\t"
        "s_add_u32 s18, s18, " STR(X2_OFF) "+3072\n\t"
        "s_add_u32 s4, s12, s18\n\t"
        "s_addc_u32 s5, s13, 0\n\t"
        "s_lshr_b32 s18, s17, 2\n\ts_lshl_b32 s18, s18, 7\n\t"
        "s_and_b32 s19, s17, 1\n\ts_lshl_b32 s19, s19, 6\n\t"
        "s_and_b32 s20, s17, 2\n\ts_lshl_b32 s20, s20, 3\n\t"
        "s_add_u32 s18, s18, s19\n\ts_add_u32 s18, s18, s20\n\t"
        "s_add_u32 s14, s14, s18\n\ts_addc_u32 s15, s15, 0\n\t"
        "v_lshlrev_b32 v3, 1, v1\n\t"
        "L_wait2:\n\t"
        "s_memrealtime s[62:63]\n\t"
        "s_waitcnt lgkmcnt(0)\n\t"
        "s_sub_u32 s62, s62, s33\n\t"
        "s_cmpk_lt_u32 s62, " STR(RDELAY) "\n\t"
        "s_cbranch_scc1 L_wait2\n\t"
        ISS7(16) "s_sleep " STR(SLP2) "\n\t" ISS7(44)
        "L_poll2:\n\t"
        CHK7(16, "L_got2a") ISS7(16) "s_sleep " STR(SLP2) "\n\t"
        CHK7(44, "L_got2b") ISS7(44) "s_sleep " STR(SLP2) "\n\t"
        "s_sub_u32 s59, s59, 1\n\t"
        "s_cbranch_scc0 L_poll2\n\t"
        "s_waitcnt vmcnt(0)\n\t"
        "L_got2a:\n\t" SUM7(16) "s_branch L_fin2\n\t"
        "L_got2b:\n\t" SUM7(44)
        "L_fin2:\n\t"
        "v_add_f32 v8, v8, v9\n\tv_add_f32 v10, v10, v11\n\t"
        "s_nop 1\n\t"
        "v_add_f32_dpp v8, v8, v8 row_ror:8 row_mask:0xf bank_mask:0xf\n\t"
        "v_add_f32_dpp v10, v10, v10 row_ror:8 row_mask:0xf bank_mask:0xf\n\t"
        "s_nop 1\n\t"
        "v_permlane16_swap_b32 v8, v10\n\t"
        "s_nop 0\n\t"
        "v_add_f32 v8, v8, v10\n\t"
        "v_mov_b32 v9, v8\n\t"
        "s_nop 1\n\t"
        "v_permlane32_swap_b32 v8, v9\n\t"
        "s_nop 0\n\t"
        "v_add_f32 v8, v8, v9\n\t"
        "v_cvt_pk_bf16_f32 v8, v8, v8\n\t"
        "s_mov_b64 exec, 0x00ff00ff\n\t"
        "global_store_short v3, v8, s[14:15]\n\t"
        "s_endpgm\n\t"
        "L_poll3:\n\t"
        "s_sleep 16\n\t"
        "buffer_load_dwordx4 v[16:19], v113, s[8:11], 0 offen offset:" STR(ACK_OFF) " sc1\n\t"
        "buffer_load_dwordx4 v[20:23], v113, s[8:11], 0 offen offset:" STR(ACK_OFF) "+1024 sc1\n\t"
        "s_sub_u32 s59, s59, 1\n\t"
        "s_waitcnt vmcnt(0)\n\t"
        X3("v8", 16) X3("v9", 20)
        "v_or_b32 v8, v8, v9\n\t"
        "v_cmp_ne_u32 vcc, 0, v8\n\t"
        "s_cbranch_vccz L_got3\n\t"
        "s_cbranch_scc0 L_poll3\n\t"
        "L_got3:\n\t"
        "s_mov_b64 exec, 1\n\t"
        "buffer_store_dword v63, off, s[8:11], 0 sc1\n\t"
        "s_endpgm\n\t"
        :
        : [ka] "s"(__builtin_amdgcn_kernarg_segment_ptr()), [bid] "s"(blockIdx.x), [tid] "v"(threadIdx.x),
          [lds] "v"((__attribute__((address_space(3))) unsigned*)lds)
        : "memory", "vcc", "scc",
          "v1", "v2", "v3", "v4", "v5", "v6", "v7", "v8", "v9", V10(1), V10(2), V10(3), V10(4), V10(5), V10(6), V10(7), V10(8), V10(9), V10(10),
          "v110", "v111", "v112", "v113", "v114", "v115",
          "s3", "s4", "s5", "s6", "s7", "s8", "s9", S10(1), S10(2), "s30", "s31", "s33", "s34", "s35", "s36", "s37", "s38", "s39",
          S10(4), S10(5), S10(6));
}

extern "C" void kernel_launch(void* const* d_in, const int* in_sizes, int n_in,
                              void* d_out, int out_size, void* d_ws, size_t ws_size,
                              hipStream_t stream) {
    glu_bilinear_decode<<<256, 512, 0, stream>>>(d_in[0], d_in[1], d_in[2], d_in[3], d_ws, d_out);
}
```

```cpp
#include <hip/hip_runtime.h>

#define NPRE 4
#define P1DELAY 19
#define SLP2 4
#define RDELAY 290

#define STR_(x) #x
#define STR(x) STR_(x)

#define ACK_OFF 1024
#define X1_OFF 4096
#define X2_OFF 0x49000
#define WS_BYTES 0xb9000
#define W_BYTES 0x700000
#define TAGC 0x9E3779B9

#define WLD(i, so) "buffer_load_dwordx4 v[16+4*" STR(i) ":19+4*" STR(i) "], v2, s[24:27], " so " offen nt\n\t"
#define DLD_(i, so, off) "buffer_load_dwordx4 v[80+4*" STR(i) ":83+4*" STR(i) "], v3, s[28:31], " so " offen offset:" off " nt\n\t"
#define DLD0 DLD_(0, "0", "0")
#define DLD1 DLD_(1, "s61", "0")
#define DLD2 DLD_(2, "s62", "0")
#define DLD3 DLD_(3, "s63", "0")
#define DLD4 DLD_(4, "0", "2048")
#define DLD5 DLD_(5, "s61", "2048")
#define DLD6 DLD_(6, "s62", "2048")
#define DLD7 DLD_(7, "s63", "2048")
#define IFPRE(i) ".if " STR(i) " < " STR(NPRE) "\n\t" DLD##i ".endif\n\t"
#define IFPOST(i) ".if " STR(i) " >= " STR(NPRE) "\n\t" DLD##i ".endif\n\t"
#define MKB(i) "v_mov_b32 v7, s[36+2*" STR(i) "]\n\t" \
    "v_perm_b32 v[48+4*" STR(i) "], s[37+2*" STR(i) "], v7, v12\n\tv_perm_b32 v[49+4*" STR(i) "], s[37+2*" STR(i) "], v7, v13\n\t" \
    "v_perm_b32 v[50+4*" STR(i) "], s[37+2*" STR(i) "], v7, v14\n\tv_perm_b32 v[51+4*" STR(i) "], s[37+2*" STR(i) "], v7, v15\n\t"
#define MM1(i, c) "s_waitcnt vmcnt(7-" STR(i) "+" STR(NPRE) ")\n\t" \
    "v_mfma_f32_16x16x32_bf16 v[8:11], v[16+4*" STR(i) ":19+4*" STR(i) "], v[48+4*" STR(i) ":51+4*" STR(i) "], " c "\n\t"
#define MKH(i) "v_and_b32 v[24+4*(" STR(i) "&1)], v[16+" STR(i) "], v12\n\tv_and_b32 v[25+4*(" STR(i) "&1)], v[16+" STR(i) "], v13\n\t" \
    "v_and_b32 v[26+4*(" STR(i) "&1)], v[16+" STR(i) "], v14\n\tv_and_b32 v[27+4*(" STR(i) "&1)], v[16+" STR(i) "], v15\n\t"
#define MM2(i, c) "s_waitcnt vmcnt(7-" STR(i) ")\n\t" \
    "v_mfma_f32_16x16x32_bf16 v[8:11], v[80+4*" STR(i) ":83+4*" STR(i) "], v[24+4*(" STR(i) "&1):27+4*(" STR(i) "&1)], " c "\n\t"
#define X3(d, r) "v_bitop3_b32 " d ", v[" STR(r) "+1], v[" STR(r) "+3], s52 bitop3:0x7e\n\t"
#define GLD4(r, sa, sb) \
    "buffer_load_dwordx4 v[" STR(r) ":" STR(r) "+3], v115, s[8:11], " sa " offen sc1\n\t" \
    "buffer_load_dwordx4 v[" STR(r) "+4:" STR(r) "+7], v115, s[8:11], " sa " offen offset:2048 sc1\n\t" \
    "buffer_load_dwordx4 v[" STR(r) "+8:" STR(r) "+11], v115, s[8:11], " sb " offen sc1\n\t" \
    "buffer_load_dwordx4 v[" STR(r) "+12:" STR(r) "+15], v115, s[8:11], " sb " offen offset:2048 sc1\n\t"
#define LD7(r, off) "global_load_dwordx4 v[" STR(r) ":" STR(r) "+3], v113, s[4:5] offset:" STR(off) " sc1\n\t"
#define ISS7(r) LD7(r, -3072) LD7(r+4, -2048) LD7(r+8, -1024) LD7(r+12, 0) LD7(r+16, 1024) LD7(r+20, 2048) LD7(r+24, 3072)
#define CHK7(r, lab) "s_waitcnt vmcnt(7)\n\t" X3("v8", r) X3("v9", r+4) X3("v10", r+8) "v_or3_b32 v8, v8, v9, v10\n\t" \
    X3("v9", r+12) X3("v10", r+16) "v_or3_b32 v8, v8, v9, v10\n\t" X3("v9", r+20) X3("v10", r+24) \
    "v_or3_b32 v8, v8, v9, v10\n\tv_cmp_ne_u32 vcc, 0, v8\n\ts_cbranch_vccz " lab "\n\t"
#define AD(d, a, b) "v_add_f32 " d ", " a ", " b "\n\t"
#define VR(r) "v[" STR(r) "]"
#define SUM7(r) AD("v8", VR(r), VR(r+4)) AD("v10", VR(r+2), VR(r+6)) AD("v9", VR(r+8), VR(r+12)) AD("v11", VR(r+10), VR(r+14)) \
    AD("v8", "v8", VR(r+16)) AD("v10", "v10", VR(r+18)) AD("v9", "v9", VR(r+20)) AD("v11", "v11", VR(r+22)) \
    AD("v8", "v8", VR(r+24)) AD("v10", "v10", VR(r+26))

#define V10(a) "v" #a "0", "v" #a "1", "v" #a "2", "v" #a "3", "v" #a "4", "v" #a "5", "v" #a "6", "v" #a "7", "v" #a "8", "v" #a "9"
#define S10(a) "s" #a "0", "s" #a "1", "s" #a "2", "s" #a "3", "s" #a "4", "s" #a "5", "s" #a "6", "s" #a "7", "s" #a "8", "s" #a "9"

__global__ void __launch_bounds__(512, 4) __attribute__((aligned(4096)))
glu_bilinear_decode(const void* x, const void* wg, const void* wu, const void* wd, void* ws, void* out) {
    __shared__ __attribute__((aligned(16))) unsigned lds[1024 + 64 + 256];
    asm volatile(
        "s_load_dwordx8 s[4:11], %[ka], 0x0\n\t"
        "s_load_dwordx4 s[12:15], %[ka], 0x20\n\t"
        "s_memrealtime s[62:63]\n\t"
        "v_lshrrev_b32 v6, 6, %[tid]\n\t"
        "v_and_b32 v1, 63, %[tid]\n\t"
        "v_readfirstlane_b32 s16, v6\n\t"
        "s_and_b32 s3, %[bid], 7\n\t"
        "s_lshr_b32 s17, %[bid], 3\n\t"
        "s_cmp_gt_u32 s3, 4\n\t"
        "s_subb_u32 s21, s3, 0\n\t"
        "s_lshl_b32 s21, s21, 5\n\t"
        "s_add_u32 s21, s21, s17\n\t"
        "s_mul_i32 s22, s21, 2341\n\t"
        "s_lshr_b32 s22, s22, 16\n\t"
        "s_mul_i32 s22, s22, 223\n\t"
        "s_lshl_b32 s21, s21, 3\n\t"
        "s_sub_u32 s64, s21, s22\n\t"
        "s_add_u32 s21, s17, 224\n\t"
        "s_cmp_eq_u32 s3, 4\n\t"
        "s_cselect_b32 s64, s21, s64\n\t"
        "s_lshr_b32 s65, s64, 3\n\t"
        "s_mov_b32 s26, " STR(W_BYTES) "\n\t"
        "s_and_b32 s19, s65, 3\n\t"
        "s_cmp_lg_u32 s19, 1\n\t"
        "s_cselect_b32 s21, 32, 24\n\t"
        "s_cselect_b32 s35, s26, 0\n\t"
        "s_mul_i32 s21, s21, s16\n\t"
        "s_lshl_b32 s18, s19, 3\n\t"
        "s_and_b32 s20, s16, 4\n\t"
        "s_add_u32 s18, s18, s20\n\t"
        "s_add_u32 s18, s18, 1\n\t"
        "s_and_b32 s20, s16, 3\n\t"
        "s_lshl_b32 s22, s20, 4\n\t"
        "s_addk_i32 s22, 192\n\t"
        "s_cmp_lt_u32 s65, 28\n\t"
        "s_cselect_b32 s18, s65, s18\n\t"
        "s_cselect_b32 s21, s21, s22\n\t"
        "s_cselect_b32 s34, s26, 0\n\t"
        "s_cselect_b32 s35, s35, 0\n\t"
        "s_cmp_lt_u32 s18, 28\n\t"
        "s_cselect_b32 s26, s26, 0\n\t"
        "s_and_b32 s22, s64, 6\n\t"
        "s_lshl_b32 s22, s22, 7\n\t"
        "s_add_u32 s21, s21, s22\n\t"
        "s_mul_i32 s22, s21, 7168\n\t"
        "s_lshl_b32 s23, s18, 8\n\t"
        "s_add_u32 s22, s22, s23\n\t"
        "s_lshl_b32 s21, s21, 1\n\t"
        "s_add_u32 s23, s21, 32\n\t"
        "s_min_u32 s23, s23, 2016\n\t"
        "v_lshrrev_b32 v6, 4, v1\n\t"
        "v_and_b32 v7, 15, v1\n\t"
        "v_mul_u32_u24 v2, 7168, v6\n\t"
        "v_lshl_add_u32 v2, v7, 4, v2\n\t"
        "v_add_u32 v2, s22, v2\n\t"
        "s_mov_b32 s27, 0x20000\n\t"
        "s_movk_i32 s54, 0x7000\n\t"
        "s_mov_b32 s55, 0xe000\n\ts_mov_b32 s56, 0x15000\n\ts_mov_b32 s57, 0x1c000\n\t"
        "s_mov_b32 s58, 0x23000\n\ts_mov_b32 s59, 0x2a000\n\ts_mov_b32 s60, 0x31000\n\t"
        "s_bitcmp0_b32 s64, 0\n\t"
        "s_waitcnt lgkmcnt(0)\n\t"
        "s_cselect_b32 s24, s6, s8\n\t"
        "s_cselect_b32 s25, s7, s9\n\t"
        "s_and_b32 s25, s25, 0xffff\n\t"
        WLD(0, "0") WLD(1, "s54") WLD(2, "s55") WLD(3, "s56")
        "s_load_dwordx8 s[36:43], s[4:5], s21\n\t"
        "s_load_dwordx8 s[44:51], s[4:5], s23\n\t"
        "s_load_dword s52, s[12:13], 0x0\n\t"
        "s_mov_b32 s26, s34\n\t" WLD(4, "s57") WLD(5, "s58")
        "s_mov_b32 s26, s35\n\t" WLD(6, "s59") WLD(7, "s60")
        "s_mov_b32 s33, s62\n\t"
        "s_mov_b32 s53, s16\n\t"
        "s_cmp_lg_u32 s3, 0\n\t"
        "s_cbranch_scc1 L_roles\n\t"
        "s_bitcmp1_b32 0xa0, s16\n\t"
        "s_cselect_b32 s53, 12, s53\n\t"
        "s_bitcmp1_b32 0x50, s16\n\t"
        "s_cselect_b32 s53, 1, s53\n\t"
        "s_add_u32 s21, %[bid], s16\n\t"
        "s_cmp_eq_u32 s21, 249\n\t"
        "s_cselect_b32 s53, 14, s53\n\t"
        "L_roles:\n\t"
        "s_mov_b32 s28, s10\n\t"
        "s_and_b32 s29, s11, 0xffff\n\t"
        "s_and_b32 s21, s53, 12\n\t"
        "s_cmp_eq_u32 s21, 4\n\t"
        "s_cselect_b32 s30, " STR(W_BYTES) ", 0\n\t"
        "s_mov_b32 s31, 0x20000\n\t"
        "s_lshl_b32 s18, s3, 3\n\t"
        "s_lshr_b32 s21, s17, 2\n\t"
        "s_add_u32 s18, s18, s21\n\t"
        "s_sub_u32 s18, s18, 8\n\t"
        "s_and_b32 s19, s17, 3\n\t"
        "s_lshl2_add_u32 s19, s19, s20\n\t"
        "s_lshl_b32 s21, s18, 17\n\t"
        "s_lshl_b32 s22, s19, 7\n\t"
        "s_add_u32 s21, s21, s22\n\t"
        "v_and_b32 v3, 32, v1\n\t"
        "v_bfe_u32 v4, v1, 3, 2\n\t"
        "v_lshl_or_b32 v3, v4, 1, v3\n\t"
        "v_lshlrev_b32 v3, 11, v3\n\t"
        "v_and_b32 v4, 7, v1\n\t"
        "v_lshl_or_b32 v3, v4, 4, v3\n\t"
        "v_add_u32 v3, s21, v3\n\t"
        "s_movk_i32 s61, 0x4000\n\ts_mov_b32 s62, 0x8000\n\ts_mov_b32 s63, 0xc000\n\t"
        IFPRE(0) IFPRE(1) IFPRE(2) IFPRE(3) IFPRE(4) IFPRE(5) IFPRE(6) IFPRE(7)
        "s_mov_b32 s8, s12\n\ts_and_b32 s9, s13, 0xffff\n\ts_mov_b32 s10, " STR(WS_BYTES) "\n\ts_mov_b32 s11, 0x20000\n\t"
        "v_mul_u32_u24 v8, 0x202, v6\n\t"
        "v_add_u32 v8, 0x100, v8\n\t"
        "v_and_b32 v9, 1, v1\n\t"
        "v_lshlrev_b32 v9, 4, v9\n\t"
        "v_xor_b32 v8, 0x0c0c, v8\n\t"
        "v_lshlrev_b32 v8, v9, v8\n\t"
        "v_mov_b32 v10, 0xffff\n\t"
        "v_lshlrev_b32 v112, v9, v10\n\t"
        "v_lshrrev_b32 v9, 1, v7\n\t"
        "v_mov_b32 v10, 0x0c0c0c0c\n\t"
        "v_xor_b32 v8, v8, v10\n\t"
        "v_cmp_eq_u32 s[22:23], 0, v9\n\tv_cmp_eq_u32 s[56:57], 1, v9\n\tv_cmp_eq_u32 s[58:59], 2, v9\n\tv_cmp_eq_u32 vcc, 3, v9\n\t"
        "v_cndmask_b32 v12, v10, v8, s[22:23]\n\tv_cndmask_b32 v13, v10, v8, s[56:57]\n\t"
        "v_cndmask_b32 v14, v10, v8, s[58:59]\n\tv_cndmask_b32 v15, v10, v8, vcc\n\t"
        "v_lshlrev_b32 v4, 4, v4\n\t"
        "v_lshl_add_u32 v4, v6, 7, v4\n\t"
        "s_lshl_b32 s21, s16, 9\n\t"
        "v_add_u32 v5, s21, v4\n\t"
        "v_add_u32 v4, 0x1100, v4\n\t"
        "v_cmp_gt_u32 vcc, 8, v7\n\t"
        "v_lshlrev_b32 v113, 4, v1\n\t"
        "s_lshl_b32 s21, s64, 10\n\t"
        "v_cndmask_b32 v4, v4, v5, vcc\n\t"
        "v_lshlrev_b32 v5, 3, v1\n\t"
        "s_addk_i32 s21, " STR(X1_OFF) "\n\t"
        "s_waitcnt lgkmcnt(0)\n\t"
        "s_add_u32 s52, s52, " STR(TAGC) "\n\t"
        MKB(0) MKB(1) MKB(2) MKB(3) MKB(4) MKB(5) MKB(6) MKB(7)
        "s_cmp_lg_u32 s53, 0\n\t"
        MM1(0, "0") MM1(1, "v[8:11]") MM1(2, "v[8:11]") MM1(3, "v[8:11]") MM1(4, "v[8:11]") MM1(5, "v[8:11]") MM1(6, "v[8:11]") MM1(7, "v[8:11]")
        "v_mov_b32 v61, s52\n\tv_mov_b32 v63, s52\n\t"
        "s_nop 5\n\t"
        "ds_write_b128 v4, v[8:11]\n\t"
        "s_waitcnt lgkmcnt(0)\n\t"
        "s_barrier\n\t"
        "s_cbranch_scc1 L_notpub\n\t"
        "s_cmp_lt_u32 s65, 28\n\t"
        "ds_read2st64_b64 v[16:19], v5 offset1:1\n\tds_read2st64_b64 v[20:23], v5 offset0:2 offset1:3\n\t"
        "ds_read2st64_b64 v[24:27], v5 offset0:4 offset1:5\n\tds_read2st64_b64 v[28:31], v5 offset0:6 offset1:7\n\t"
        "s_waitcnt lgkmcnt(2)\n\t"
        "v_add_f32 v16, v16, v18\n\tv_add_f32 v17, v17, v19\n\tv_add_f32 v20, v20, v22\n\tv_add_f32 v21, v21, v23\n\t"
        "v_add_f32 v16, v16, v20\n\tv_add_f32 v17, v17, v21\n\t"
        "s_waitcnt lgkmcnt(0)\n\t"
        "v_add_f32 v24, v24, v26\n\tv_add_f32 v25, v25, v27\n\tv_add_f32 v28, v28, v30\n\tv_add_f32 v29, v29, v31\n\t"
        "v_add_f32 v24, v24, v28\n\tv_add_f32 v25, v25, v29\n\t"
        "s_cbranch_scc0 L_two\n\t"
        "v_add_f32 v60, v16, v24\n\tv_add_f32 v62, v17, v25\n\t"
        "L_store:\n\t"
        "buffer_store_dwordx4 v[60:63], v113, s[8:11], s21 offen sc1\n\t"
        "s_barrier\n\t"
        "s_cmp_eq_u32 s3, 0\n\t"
        "s_cbranch_scc1 L_end0\n\t"
        "v_and_b32 v8, 31, v1\n\t"
        "v_lshrrev_b32 v9, 5, v1\n\t"
        "v_lshlrev_b32 v115, 4, v8\n\t"
        "v_lshl_add_u32 v115, v9, 10, v115\n\t"
        "v_lshlrev_b32 v114, 3, v8\n\t"
        "v_lshl_add_u32 v114, v9, 2, v114\n\t"
        "s_and_b32 s66, s18, 1\n\t"
        "s_lshl_b32 s66, s66, 9\n\t"
        "s_addk_i32 s66, " STR(X1_OFF) "\n\t"
        "s_lshr_b32 s64, s18, 1\n\t"
        "s_and_b32 s68, s64, 3\n\t"
        "s_lshl_b32 s64, s64, 13\n\t"
        "s_add_u32 s64, s64, s66\n\t"
        "s_add_u32 s65, s64, 4096\n\t"
        "s_lshr_b32 s67, s18, 3\n\t"
        "s_lshl_b32 s67, s67, 13\n\t"
        "s_add_u32 s66, s66, s67\n\t"
        "s_add_u32 s66, s66, 224*1024\n\t"
        "s_add_u32 s67, s66, 4096\n\t"
        "s_mov_b32 s69, 0x10000\n\t"
        "v_mov_b32 v32, 0\n\tv_mov_b32 v34, 0\n\tv_mov_b32 v6, 0\n\t"
        "s_sleep " STR(P1DELAY) "\n\t"
        "L_poll1:\n\t"
        "s_cmp_lg_u32 s68, 1\n\t"
        "s_cbranch_scc1 L_p1b\n\t"
        GLD4(32, "s66", "s67")
        "L_p1b:\n\t"
        GLD4(16, "s64", "s65")
        "s_cbranch_scc1 L_p1c\n\t"
        "s_waitcnt vmcnt(4)\n\t"
        X3("v8", 32) X3("v9", 36) X3("v10", 40) X3("v11", 44)
        "v_add_f32 v32, v32, v36\n\tv_add_f32 v34, v34, v38\n\tv_add_f32 v40, v40, v44\n\tv_add_f32 v42, v42, v46\n\t"
        "v_or3_b32 v6, v8, v9, v10\n\t"
        "v_add_f32 v32, v32, v40\n\tv_add_f32 v34, v34, v42\n\t"
        "v_or_b32 v6, v6, v11\n\t"
        "L_p1c:\n\t"
        "s_sub_u32 s69, s69, 1\n\t"
        "s_waitcnt vmcnt(2)\n\t"
        X3("v8", 16) X3("v9", 20)
        "v_add_f32 v16, v16, v20\n\tv_add_f32 v18, v18, v22\n\t"
        "v_or3_b32 v8, v8, v9, v6\n\t"
        "s_waitcnt vmcnt(0)\n\t"
        "v_add_f32 v24, v24, v28\n\tv_add_f32 v26, v26, v30\n\t"
        X3("v10", 24) X3("v11", 28)
        "v_add_f32 v16, v16, v24\n\tv_add_f32 v18, v18, v26\n\t"
        "v_add_f32 v16, v16, v32\n\tv_add_f32 v18, v18, v34\n\t"
        "v_or3_b32 v8, v8, v10, v11\n\t"
        "v_cmp_ne_u32 vcc, 0, v8\n\t"
        "v_permlane32_swap_b32 v16, v18\n\t"
        "s_cbranch_vccz L_got1\n\t"
        "s_cbranch_scc0 L_poll1\n\t"
        "L_got1:\n\t"
        "v_cvt_pk_bf16_f32 v16, v16, v18\n\t"
        "v_lshlrev_b32 v18, 16, v16\n\t"
        "v_and_b32 v16, 0xffff0000, v16\n\t"
        "v_mul_f32 v16, v16, v18\n\t"
        "v_cvt_pk_bf16_f32 v16, v16, v16\n\t"
        "ds_write_b32 v114, v16 offset:4096\n\t"
        "s_waitcnt lgkmcnt(0)\n\t"
        "s_barrier\n\t"
        "s_endpgm\n\t"
        "L_two:\n\t"
        "s_sub_u32 s22, s65, 28\n\t"
        "s_lshl_b32 s22, s22, 13\n\t"
        "s_add_u32 s21, s21, s22\n\t"
        "v_mov_b32 v18, v17\n\tv_mov_b32 v17, s52\n\tv_mov_b32 v19, s52\n\t"
        "buffer_store_dwordx4 v[16:19], v113, s[8:11], s21 offen sc1\n\t"
        "s_addk_i32 s21, 8192\n\t"
        "v_mov_b32 v60, v24\n\tv_mov_b32 v62, v25\n\t"
        "s_branch L_store\n\t"
        "L_ack:\n\t"
        "s_lshl_b32 s21, %[bid], 3\n\t"
        "s_mov_b64 exec, 1\n\t"
        "buffer_store_dwordx2 v[62:63], off, s[8:11], s21 offset:" STR(ACK_OFF) " sc1\n\t"
        "L_end0:\n\t"
        "s_endpgm\n\t"
        "L_notpub:\n\t"
        "s_cmp_lt_u32 s53, 3\n\t"
        "s_cbranch_scc1 L_end0\n\t"
        "s_cmp_eq_u32 s53, 3\n\t"
        "s_barrier\n\t"
        "s_cbranch_scc1 L_ack\n\t"
        "s_cmp_gt_u32 s53, 7\n\t"
        "s_cbranch_scc1 L_notdown\n\t"
        IFPOST(0) IFPOST(1) IFPOST(2) IFPOST(3) IFPOST(4) IFPOST(5) IFPOST(6) IFPOST(7)
        "v_bfe_u32 v9, v1, 1, 2\n\t"
        "v_cmp_eq_u32 s[22:23], 0, v9\n\tv_cmp_eq_u32 s[56:57], 1, v9\n\tv_cmp_eq_u32 s[58:59], 2, v9\n\tv_cmp_eq_u32 vcc, 3, v9\n\t"
        "v_cndmask_b32 v12, 0, v112, s[22:23]\n\tv_cndmask_b32 v13, 0, v112, s[56:57]\n\t"
        "v_cndmask_b32 v14, 0, v112, s[58:59]\n\tv_cndmask_b32 v15, 0, v112, vcc\n\t"
        "v_and_b32 v5, 56, v1\n\t"
        "v_lshlrev_b32 v5, 2, v5\n\t"
        "s_mul_i32 s21, s19, 4*7168\n\t"
        "s_lshl_b32 s22, s18, 7\n\t"
        "s_add_u32 s21, s21, s22\n\t"
        "s_add_u32 s21, s21, " STR(X2_OFF) "\n\t"
        "v_mul_u32_u24 v4, 7168, v6\n\t"
        "v_and_b32 v7, 7, v1\n\t"
        "v_lshl_add_u32 v4, v7, 4, v4\n\t"
        "s_mov_b32 s22, 0x00ff00ff\n\ts_mov_b32 s23, 0x00ff00ff\n\t"
        "s_barrier\n\t"
        "ds_read_b128 v[16:19], v5 offset:4096\n\t"
        "ds_read_b128 v[20:23], v5 offset:4112\n\t"
        "s_waitcnt lgkmcnt(1)\n\t"
        MKH(0) MKH(1) MM2(0, "0") MKH(2) MM2(1, "v[8:11]") MKH(3) MM2(2, "v[8:11]") "s_waitcnt lgkmcnt(0)\n\t" MKH(4) MM2(3, "v[8:11]")
        MKH(5) MM2(4, "v[8:11]") MKH(6) MM2(5, "v[8:11]") MKH(7) MM2(6, "v[8:11]") "s_nop 0\n\t" MM2(7, "v[8:11]")
        "s_nop 7\n\t"
        "v_permlane32_swap_b32 v8, v9\n\t"
        "v_permlane32_swap_b32 v10, v11\n\t"
        "s_nop 1\n\t"
        "v_add_f32_dpp v60, v9, v8 row_ror:8 row_mask:0xf bank_mask:0xf\n\t"
        "v_add_f32_dpp v62, v11, v10 row_ror:8 row_mask:0xf bank_mask:0xf\n\t"
        "s_mov_b64 exec, s[22:23]\n\t"
        "buffer_store_dwordx4 v[60:63], v4, s[8:11], s21 offen sc1\n\t"
        "s_endpgm\n\t"
        "L_notdown:\n\t"
        "s_mov_b32 s59, 0x10000\n\t"
        "s_cmp_eq_u32 s53, 14\n\t"
        "s_cbranch_scc1 L_poll3\n\t"
        "s_lshl_b32 s17, s17, 1\n\t"
        "s_bfe_u32 s18, s16, 0x10001\n\t"
        "s_add_u32 s17, s17, s18\n\t"
        "s_mul_i32 s18, s17, 7168\n\t"
        "s_add_u32 s18, s18, " STR(X2_OFF) "+3072\n\t"
        "s_add_u32 s4, s12, s18\n\t"
        "s_addc_u32 s5, s13, 0\n\t"
        "s_lshr_b32 s18, s17, 2\n\ts_lshl_b32 s18, s18, 7\n\t"
        "s_and_b32 s19, s17, 1\n\ts_lshl_b32 s19, s19, 6\n\t"
        "s_and_b32 s20, s17, 2\n\ts_lshl_b32 s20, s20, 3\n\t"
        "s_add_u32 s18, s18, s19\n\ts_add_u32 s18, s18, s20\n\t"
        "s_add_u32 s14, s14, s18\n\ts_addc_u32 s15, s15, 0\n\t"
        "v_lshlrev_b32 v3, 1, v1\n\t"
        "L_wait2:\n\t"
        "s_memrealtime s[62:63]\n\t"
        "s_waitcnt lgkmcnt(0)\n\t"
        "s_sub_u32 s62, s62, s33\n\t"
        "s_cmpk_lt_u32 s62, " STR(RDELAY) "\n\t"
        "s_cbranch_scc1 L_wait2\n\t"
        ISS7(16) "s_sleep " STR(SLP2) "\n\t" ISS7(44)
        "L_poll2:\n\t"
        CHK7(16, "L_got2a") ISS7(16) "s_sleep " STR(SLP2) "\n\t"
        CHK7(44, "L_got2b") ISS7(44) "s_sleep " STR(SLP2) "\n\t"
        "s_sub_u32 s59, s59, 1\n\t"
        "s_cbranch_scc0 L_poll2\n\t"
        "s_waitcnt vmcnt(0)\n\t"
        "L_got2a:\n\t" SUM7(16) "s_branch L_fin2\n\t"
        "L_got2b:\n\t" SUM7(44)
        "L_fin2:\n\t"
        "v_add_f32 v8, v8, v9\n\tv_add_f32 v10, v10, v11\n\t"
        "s_nop 1\n\t"
        "v_add_f32_dpp v8, v8, v8 row_ror:8 row_mask:0xf bank_mask:0xf\n\t"
        "v_add_f32_dpp v10, v10, v10 row_ror:8 row_mask:0xf bank_mask:0xf\n\t"
        "s_nop 1\n\t"
        "v_permlane16_swap_b32 v8, v10\n\t"
        "s_nop 0\n\t"
        "v_add_f32 v8, v8, v10\n\t"
        "v_mov_b32 v9, v8\n\t"
        "s_nop 1\n\t"
        "v_permlane32_swap_b32 v8, v9\n\t"
        "s_nop 0\n\t"
        "v_add_f32 v8, v8, v9\n\t"
        "v_cvt_pk_bf16_f32 v8, v8, v8\n\t"
        "s_mov_b64 exec, 0x00ff00ff\n\t"
        "global_store_short v3, v8, s[14:15]\n\t"
        "s_endpgm\n\t"
        "L_poll3:\n\t"
        "s_sleep 16\n\t"
        "buffer_load_dwordx4 v[16:19], v113, s[8:11], 0 offen offset:" STR(ACK_OFF) " sc1\n\t"
        "buffer_load_dwordx4 v[20:23], v113, s[8:11], 0 offen offset:" STR(ACK_OFF) "+1024 sc1\n\t"
        "s_sub_u32 s59, s59, 1\n\t"
        "s_waitcnt vmcnt(0)\n\t"
        X3("v8", 16) X3("v9", 20)
        "v_or_b32 v8, v8, v9\n\t"
        "v_cmp_ne_u32 vcc, 0, v8\n\t"
        "s_cbranch_vccz L_got3\n\t"
        "s_cbranch_scc0 L_poll3\n\t"
        "L_got3:\n\t"
        "s_mov_b64 exec, 1\n\t"
        "buffer_store_dword v63, off, s[8:11], 0 sc1\n\t"
        "s_endpgm\n\t"
        :
        : [ka] "s"(__builtin_amdgcn_kernarg_segment_ptr()), [bid] "s"(blockIdx.x), [tid] "v"(threadIdx.x),
          [lds] "v"((__attribute__((address_space(3))) unsigned*)lds)
        : "memory", "vcc", "scc",
          "v1", "v2", "v3", "v4", "v5", "v6", "v7", "v8", "v9", V10(1), V10(2), V10(3), V10(4), V10(5), V10(6), V10(7), V10(8), V10(9), V10(10),
          "v110", "v111", "v112", "v113", "v114", "v115",
          "s3", "s4", "s5", "s6", "s7", "s8", "s9", S10(1), S10(2), "s30", "s31", "s33", "s34", "s35", "s36", "s37", "s38", "s39",
          S10(4), S10(5), S10(6));
}

extern "C" void kernel_launch(void* const* d_in, const int* in_sizes, int n_in,
                              void* d_out, int out_size, void* d_ws, size_t ws_size,
                              hipStream_t stream) {
    glu_bilinear_decode<<<256, 512, 0, stream>>>(d_in[0], d_in[1], d_in[2], d_in[3], d_ws, d_out);
}
```

```cpp
#include <hip/hip_runtime.h>

#define NPRE 4
#define XDELAY 6
#define P1DELAY 19
#define SLP2 4
#define RDELAY 290

#define STR_(x) #x
#define STR(x) STR_(x)

#define ACK_OFF 1024
#define X1_OFF 4096
#define X2_OFF 0x49000
#define WS_BYTES 0xb9000
#define W_BYTES 0x700000
#define TAGC 0x9E3779B9

#define WLD(i, so) "buffer_load_dwordx4 v[16+4*" STR(i) ":19+4*" STR(i) "], v2, s[24:27], " so " offen nt\n\t"
#define XLD "s_load_dwordx8 s[36:43], s[4:5], s21\n\ts_load_dwordx8 s[44:51], s[4:5], s23\n\t"
#define DLD_(i, so, off) "buffer_load_dwordx4 v[80+4*" STR(i) ":83+4*" STR(i) "], v3, s[28:31], " so " offen offset:" off " nt\n\t"
#define DLD0 DLD_(0, "0", "0")
#define DLD1 DLD_(1, "s61", "0")
#define DLD2 DLD_(2, "s62", "0")
#define DLD3 DLD_(3, "s63", "0")
#define DLD4 DLD_(4, "0", "2048")
#define DLD5 DLD_(5, "s61", "2048")
#define DLD6 DLD_(6, "s62", "2048")
#define DLD7 DLD_(7, "s63", "2048")
#define IFPRE(i) ".if " STR(i) " < " STR(NPRE) "\n\t" DLD##i ".endif\n\t"
#define IFPOST(i) ".if " STR(i) " >= " STR(NPRE) "\n\t" DLD##i ".endif\n\t"
#define MKB(i) "v_mov_b32 v7, s[36+2*" STR(i) "]\n\t" \
    "v_perm_b32 v[48+4*" STR(i) "], s[37+2*" STR(i) "], v7, v12\n\tv_perm_b32 v[49+4*" STR(i) "], s[37+2*" STR(i) "], v7, v13\n\t" \
    "v_perm_b32 v[50+4*" STR(i) "], s[37+2*" STR(i) "], v7, v14\n\tv_perm_b32 v[51+4*" STR(i) "], s[37+2*" STR(i) "], v7, v15\n\t"
#define MM1(i, c) "s_waitcnt vmcnt(7-" STR(i) "+" STR(NPRE) ")\n\t" \
    "v_mfma_f32_16x16x32_bf16 v[8:11], v[16+4*" STR(i) ":19+4*" STR(i) "], v[48+4*" STR(i) ":51+4*" STR(i) "], " c "\n\t"
#define MKH(i) "v_and_b32 v[24+4*(" STR(i) "&1)], v[16+" STR(i) "], v12\n\tv_and_b32 v[25+4*(" STR(i) "&1)], v[16+" STR(i) "], v13\n\t" \
    "v_and_b32 v[26+4*(" STR(i) "&1)], v[16+" STR(i) "], v14\n\tv_and_b32 v[27+4*(" STR(i) "&1)], v[16+" STR(i) "], v15\n\t"
#define MM2(i, c) "s_waitcnt vmcnt(7-" STR(i) ")\n\t" \
    "v_mfma_f32_16x16x32_bf16 v[8:11], v[80+4*" STR(i) ":83+4*" STR(i) "], v[24+4*(" STR(i) "&1):27+4*(" STR(i) "&1)], " c "\n\t"
#define X3(d, r) "v_bitop3_b32 " d ", v[" STR(r) "+1], v[" STR(r) "+3], s52 bitop3:0x7e\n\t"
#define GLD4(r, sa, sb) \
    "buffer_load_dwordx4 v[" STR(r) ":" STR(r) "+3], v115, s[8:11], " sa " offen sc1\n\t" \
    "buffer_load_dwordx4 v[" STR(r) "+4:" STR(r) "+7], v115, s[8:11], " sa " offen offset:2048 sc1\n\t" \
    "buffer_load_dwordx4 v[" STR(r) "+8:" STR(r) "+11], v115, s[8:11], " sb " offen sc1\n\t" \
    "buffer_load_dwordx4 v[" STR(r) "+12:" STR(r) "+15], v115, s[8:11], " sb " offen offset:2048 sc1\n\t"
#define LD7(r, off) "global_load_dwordx4 v[" STR(r) ":" STR(r) "+3], v113, s[4:5] offset:" STR(off) " sc1\n\t"
#define ISS7(r) LD7(r, -3072) LD7(r+4, -2048) LD7(r+8, -1024) LD7(r+12, 0) LD7(r+16, 1024) LD7(r+20, 2048) LD7(r+24, 3072)
#define CHK7(r, lab) "s_waitcnt vmcnt(7)\n\t" X3("v8", r) X3("v9", r+4) X3("v10", r+8) "v_or3_b32 v8, v8, v9, v10\n\t" \
    X3("v9", r+12) X3("v10", r+16) "v_or3_b32 v8, v8, v9, v10\n\t" X3("v9", r+20) X3("v10", r+24) \
    "v_or3_b32 v8, v8, v9, v10\n\tv_cmp_ne_u32 vcc, 0, v8\n\ts_cbranch_vccz " lab "\n\t"
#define AD(d, a, b) "v_add_f32 " d ", " a ", " b "\n\t"
#define VR(r) "v[" STR(r) "]"
#define SUM7(r) AD("v8", VR(r), VR(r+4)) AD("v10", VR(r+2), VR(r+6)) AD("v9", VR(r+8), VR(r+12)) AD("v11", VR(r+10), VR(r+14)) \
    AD("v8", "v8", VR(r+16)) AD("v10", "v10", VR(r+18)) AD("v9", "v9", VR(r+20)) AD("v11", "v11", VR(r+22)) \
    AD("v8", "v8", VR(r+24)) AD("v10", "v10", VR(r+26))

#define V10(a) "v" #a "0", "v" #a "1", "v" #a "2", "v" #a "3", "v" #a "4", "v" #a "5", "v" #a "6", "v" #a "7", "v" #a "8", "v" #a "9"
#define S10(a) "s" #a "0", "s" #a "1", "s" #a "2", "s" #a "3", "s" #a "4", "s" #a "5", "s" #a "6", "s" #a "7", "s" #a "8", "s" #a "9"

__global__ void __launch_bounds__(512, 4) __attribute__((aligned(4096)))
glu_bilinear_decode(const void* x, const void* wg, const void* wu, const void* wd, void* ws, void* out) {
    __shared__ __attribute__((aligned(16))) unsigned lds[1024 + 64 + 256];
    asm volatile(
        "s_load_dwordx8 s[4:11], %[ka], 0x0\n\t"
        "s_load_dwordx4 s[12:15], %[ka], 0x20\n\t"
        "s_memrealtime s[62:63]\n\t"
        "v_lshrrev_b32 v6, 6, %[tid]\n\t"
        "v_and_b32 v1, 63, %[tid]\n\t"
        "v_readfirstlane_b32 s16, v6\n\t"
        "s_and_b32 s3, %[bid], 7\n\t"
        "s_lshr_b32 s17, %[bid], 3\n\t"
        "s_cmp_gt_u32 s3, 4\n\t"
        "s_subb_u32 s21, s3, 0\n\t"
        "s_lshl_b32 s21, s21, 5\n\t"
        "s_add_u32 s21, s21, s17\n\t"
        "s_mul_i32 s22, s21, 2341\n\t"
        "s_lshr_b32 s22, s22, 16\n\t"
        "s_mul_i32 s22, s22, 223\n\t"
        "s_lshl_b32 s21, s21, 3\n\t"
        "s_sub_u32 s64, s21, s22\n\t"
        "s_add_u32 s21, s17, 224\n\t"
        "s_cmp_eq_u32 s3, 4\n\t"
        "s_cselect_b32 s64, s21, s64\n\t"
        "s_lshr_b32 s65, s64, 3\n\t"
        "s_mov_b32 s26, " STR(W_BYTES) "\n\t"
        "s_and_b32 s19, s65, 3\n\t"
        "s_cmp_lg_u32 s19, 1\n\t"
        "s_cselect_b32 s21, 32, 24\n\t"
        "s_cselect_b32 s35, s26, 0\n\t"
        "s_mul_i32 s21, s21, s16\n\t"
        "s_lshl_b32 s18, s19, 3\n\t"
        "s_and_b32 s20, s16, 4\n\t"
        "s_add_u32 s18, s18, s20\n\t"
        "s_add_u32 s18, s18, 1\n\t"
        "s_and_b32 s20, s16, 3\n\t"
        "s_lshl_b32 s22, s20, 4\n\t"
        "s_addk_i32 s22, 192\n\t"
        "s_cmp_lt_u32 s65, 28\n\t"
        "s_cselect_b32 s18, s65, s18\n\t"
        "s_cselect_b32 s21, s21, s22\n\t"
        "s_cselect_b32 s34, s26, 0\n\t"
        "s_cselect_b32 s35, s35, 0\n\t"
        "s_cmp_lt_u32 s18, 28\n\t"
        "s_cselect_b32 s26, s26, 0\n\t"
        "s_and_b32 s22, s64, 6\n\t"
        "s_lshl_b32 s22, s22, 7\n\t"
        "s_add_u32 s21, s21, s22\n\t"
        "s_mul_i32 s22, s21, 7168\n\t"
        "s_lshl_b32 s23, s18, 8\n\t"
        "s_add_u32 s22, s22, s23\n\t"
        "s_lshl_b32 s21, s21, 1\n\t"
        "s_add_u32 s23, s21, 32\n\t"
        "s_min_u32 s23, s23, 2016\n\t"
        "v_lshrrev_b32 v6, 4, v1\n\t"
        "v_and_b32 v7, 15, v1\n\t"
        "v_mul_u32_u24 v2, 7168, v6\n\t"
        "v_lshl_add_u32 v2, v7, 4, v2\n\t"
        "v_add_u32 v2, s22, v2\n\t"
        "s_mov_b32 s27, 0x20000\n\t"
        "s_movk_i32 s54, 0x7000\n\t"
        "s_mov_b32 s55, 0xe000\n\ts_mov_b32 s56, 0x15000\n\ts_mov_b32 s57, 0x1c000\n\t"
        "s_mov_b32 s58, 0x23000\n\ts_mov_b32 s59, 0x2a000\n\ts_mov_b32 s60, 0x31000\n\t"
        "s_bitcmp0_b32 s64, 0\n\t"
        "s_waitcnt lgkmcnt(0)\n\t"
        "s_cselect_b32 s24, s6, s8\n\t"
        "s_cselect_b32 s25, s7, s9\n\t"
        "s_and_b32 s25, s25, 0xffff\n\t"
        WLD(0, "0") WLD(1, "s54") WLD(2, "s55") WLD(3, "s56")
        "s_load_dword s52, s[12:13], 0x0\n\t"
        "s_cmp_lt_u32 s16, 4\n\t"
        "s_cbranch_scc1 L_nox\n\t"
        XLD
        "L_nox:\n\t"
        "s_mov_b32 s26, s34\n\t" WLD(4, "s57") WLD(5, "s58")
        "s_mov_b32 s26, s35\n\t" WLD(6, "s59") WLD(7, "s60")
        "s_cbranch_scc0 L_hasx\n\t"
        "s_sleep " STR(XDELAY) "\n\t"
        XLD
        "L_hasx:\n\t"
        "s_mov_b32 s33, s62\n\t"
        "s_mov_b32 s53, s16\n\t"
        "s_cmp_lg_u32 s3, 0\n\t"
        "s_cbranch_scc1 L_roles\n\t"
        "s_bitcmp1_b32 0xa0, s16\n\t"
        "s_cselect_b32 s53, 12, s53\n\t"
        "s_bitcmp1_b32 0x50, s16\n\t"
        "s_cselect_b32 s53, 1, s53\n\t"
        "s_add_u32 s21, %[bid], s16\n\t"
        "s_cmp_eq_u32 s21, 249\n\t"
        "s_cselect_b32 s53, 14, s53\n\t"
        "L_roles:\n\t"
        "s_mov_b32 s28, s10\n\t"
        "s_and_b32 s29, s11, 0xffff\n\t"
        "s_and_b32 s21, s53, 12\n\t"
        "s_cmp_eq_u32 s21, 4\n\t"
        "s_cselect_b32 s30, " STR(W_BYTES) ", 0\n\t"
        "s_mov_b32 s31, 0x20000\n\t"
        "s_lshl_b32 s18, s3, 3\n\t"
        "s_lshr_b32 s21, s17, 2\n\t"
        "s_add_u32 s18, s18, s21\n\t"
        "s_sub_u32 s18, s18, 8\n\t"
        "s_and_b32 s19, s17, 3\n\t"
        "s_lshl2_add_u32 s19, s19, s20\n\t"
        "s_lshl_b32 s21, s18, 17\n\t"
        "s_lshl_b32 s22, s19, 7\n\t"
        "s_add_u32 s21, s21, s22\n\t"
        "v_and_b32 v3, 32, v1\n\t"
        "v_bfe_u32 v4, v1, 3, 2\n\t"
        "v_lshl_or_b32 v3, v4, 1, v3\n\t"
        "v_lshlrev_b32 v3, 11, v3\n\t"
        "v_and_b32 v4, 7, v1\n\t"
        "v_lshl_or_b32 v3, v4, 4, v3\n\t"
        "v_add_u32 v3, s21, v3\n\t"
        "s_movk_i32 s61, 0x4000\n\ts_mov_b32 s62, 0x8000\n\ts_mov_b32 s63, 0xc000\n\t"
        IFPRE(0) IFPRE(1) IFPRE(2) IFPRE(3) IFPRE(4) IFPRE(5) IFPRE(6) IFPRE(7)
        "s_mov_b32 s8, s12\n\ts_and_b32 s9, s13, 0xffff\n\ts_mov_b32 s10, " STR(WS_BYTES) "\n\ts_mov_b32 s11, 0x20000\n\t"
        "v_mul_u32_u24 v8, 0x202, v6\n\t"
        "v_add_u32 v8, 0x100, v8\n\t"
        "v_and_b32 v9, 1, v1\n\t"
        "v_lshlrev_b32 v9, 4, v9\n\t"
        "v_xor_b32 v8, 0x0c0c, v8\n\t"
        "v_lshlrev_b32 v8, v9, v8\n\t"
        "v_mov_b32 v10, 0xffff\n\t"
        "v_lshlrev_b32 v112, v9, v10\n\t"
        "v_lshrrev_b32 v9, 1, v7\n\t"
        "v_mov_b32 v10, 0x0c0c0c0c\n\t"
        "v_xor_b32 v8, v8, v10\n\t"
        "v_cmp_eq_u32 s[22:23], 0, v9\n\tv_cmp_eq_u32 s[56:57], 1, v9\n\tv_cmp_eq_u32 s[58:59], 2, v9\n\tv_cmp_eq_u32 vcc, 3, v9\n\t"
        "v_cndmask_b32 v12, v10, v8, s[22:23]\n\tv_cndmask_b32 v13, v10, v8, s[56:57]\n\t"
        "v_cndmask_b32 v14, v10, v8, s[58:59]\n\tv_cndmask_b32 v15, v10, v8, vcc\n\t"
        "v_lshlrev_b32 v4, 4, v4\n\t"
        "v_lshl_add_u32 v4, v6, 7, v4\n\t"
        "s_lshl_b32 s21, s16, 9\n\t"
        "v_add_u32 v5, s21, v4\n\t"
        "v_add_u32 v4, 0x1100, v4\n\t"
        "v_cmp_gt_u32 vcc, 8, v7\n\t"
        "v_lshlrev_b32 v113, 4, v1\n\t"
        "s_lshl_b32 s21, s64, 10\n\t"
        "v_cndmask_b32 v4, v4, v5, vcc\n\t"
        "v_lshlrev_b32 v5, 3, v1\n\t"
        "s_addk_i32 s21, " STR(X1_OFF) "\n\t"
        "s_waitcnt lgkmcnt(0)\n\t"
        "s_add_u32 s52, s52, " STR(TAGC) "\n\t"
        MKB(0) MKB(1) MKB(2) MKB(3) MKB(4) MKB(5) MKB(6) MKB(7)
        "s_cmp_lg_u32 s53, 0\n\t"
        MM1(0, "0") MM1(1, "v[8:11]") MM1(2, "v[8:11]") MM1(3, "v[8:11]") MM1(4, "v[8:11]") MM1(5, "v[8:11]") MM1(6, "v[8:11]") MM1(7, "v[8:11]")
        "v_mov_b32 v61, s52\n\tv_mov_b32 v63, s52\n\t"
        "s_nop 5\n\t"
        "ds_write_b128 v4, v[8:11]\n\t"
        "s_waitcnt lgkmcnt(0)\n\t"
        "s_barrier\n\t"
        "s_cbranch_scc1 L_notpub\n\t"
        "s_cmp_lt_u32 s65, 28\n\t"
        "ds_read2st64_b64 v[16:19], v5 offset1:1\n\tds_read2st64_b64 v[20:23], v5 offset0:2 offset1:3\n\t"
        "ds_read2st64_b64 v[24:27], v5 offset0:4 offset1:5\n\tds_read2st64_b64 v[28:31], v5 offset0:6 offset1:7\n\t"
        "s_waitcnt lgkmcnt(2)\n\t"
        "v_add_f32 v16, v16, v18\n\tv_add_f32 v17, v17, v19\n\tv_add_f32 v20, v20, v22\n\tv_add_f32 v21, v21, v23\n\t"
        "v_add_f32 v16, v16, v20\n\tv_add_f32 v17, v17, v21\n\t"
        "s_waitcnt lgkmcnt(0)\n\t"
        "v_add_f32 v24, v24, v26\n\tv_add_f32 v25, v25, v27\n\tv_add_f32 v28, v28, v30\n\tv_add_f32 v29, v29, v31\n\t"
        "v_add_f32 v24, v24, v28\n\tv_add_f32 v25, v25, v29\n\t"
        "s_cbranch_scc0 L_two\n\t"
        "v_add_f32 v60, v16, v24\n\tv_add_f32 v62, v17, v25\n\t"
        "L_store:\n\t"
        "buffer_store_dwordx4 v[60:63], v113, s[8:11], s21 offen sc1\n\t"
        "s_barrier\n\t"
        "s_cmp_eq_u32 s3, 0\n\t"
        "s_cbranch_scc1 L_end0\n\t"
        "v_and_b32 v8, 31, v1\n\t"
        "v_lshrrev_b32 v9, 5, v1\n\t"
        "v_lshlrev_b32 v115, 4, v8\n\t"
        "v_lshl_add_u32 v115, v9, 10, v115\n\t"
        "v_lshlrev_b32 v114, 3, v8\n\t"
        "v_lshl_add_u32 v114, v9, 2, v114\n\t"
        "s_and_b32 s66, s18, 1\n\t"
        "s_lshl_b32 s66, s66, 9\n\t"
        "s_addk_i32 s66, " STR(X1_OFF) "\n\t"
        "s_lshr_b32 s64, s18, 1\n\t"
        "s_and_b32 s68, s64, 3\n\t"
        "s_lshl_b32 s64, s64, 13\n\t"
        "s_add_u32 s64, s64, s66\n\t"
        "s_add_u32 s65, s64, 4096\n\t"
        "s_lshr_b32 s67, s18, 3\n\t"
        "s_lshl_b32 s67, s67, 13\n\t"
        "s_add_u32 s66, s66, s67\n\t"
        "s_add_u32 s66, s66, 224*1024\n\t"
        "s_add_u32 s67, s66, 4096\n\t"
        "s_mov_b32 s69, 0x10000\n\t"
        "v_mov_b32 v32, 0\n\tv_mov_b32 v34, 0\n\tv_mov_b32 v6, 0\n\t"
        "s_sleep " STR(P1DELAY) "\n\t"
        "L_poll1:\n\t"
        "s_cmp_lg_u32 s68, 1\n\t"
        "s_cbranch_scc1 L_p1b\n\t"
        GLD4(32, "s66", "s67")
        "L_p1b:\n\t"
        GLD4(16, "s64", "s65")
        "s_cbranch_scc1 L_p1c\n\t"
        "s_waitcnt vmcnt(4)\n\t"
        X3("v8", 32) X3("v9", 36) X3("v10", 40) X3("v11", 44)
        "v_add_f32 v32, v32, v36\n\tv_add_f32 v34, v34, v38\n\tv_add_f32 v40, v40, v44\n\tv_add_f32 v42, v42, v46\n\t"
        "v_or3_b32 v6, v8, v9, v10\n\t"
        "v_add_f32 v32, v32, v40\n\tv_add_f32 v34, v34, v42\n\t"
        "v_or_b32 v6, v6, v11\n\t"
        "L_p1c:\n\t"
        "s_sub_u32 s69, s69, 1\n\t"
        "s_waitcnt vmcnt(2)\n\t"
        X3("v8", 16) X3("v9", 20)
        "v_add_f32 v16, v16, v20\n\tv_add_f32 v18, v18, v22\n\t"
        "v_or3_b32 v8, v8, v9, v6\n\t"
        "s_waitcnt vmcnt(0)\n\t"
        "v_add_f32 v24, v24, v28\n\tv_add_f32 v26, v26, v30\n\t"
        X3("v10", 24) X3("v11", 28)
        "v_add_f32 v16, v16, v24\n\tv_add_f32 v18, v18, v26\n\t"
        "v_add_f32 v16, v16, v32\n\tv_add_f32 v18, v18, v34\n\t"
        "v_or3_b32 v8, v8, v10, v11\n\t"
        "v_cmp_ne_u32 vcc, 0, v8\n\t"
        "v_permlane32_swap_b32 v16, v18\n\t"
        "s_cbranch_vccz L_got1\n\t"
        "s_cbranch_scc0 L_poll1\n\t"
        "L_got1:\n\t"
        "v_cvt_pk_bf16_f32 v16, v16, v18\n\t"
        "v_lshlrev_b32 v18, 16, v16\n\t"
        "v_and_b32 v16, 0xffff0000, v16\n\t"
        "v_mul_f32 v16, v16, v18\n\t"
        "v_cvt_pk_bf16_f32 v16, v16, v16\n\t"
        "ds_write_b32 v114, v16 offset:4096\n\t"
        "s_waitcnt lgkmcnt(0)\n\t"
        "s_barrier\n\t"
        "s_endpgm\n\t"
        "L_two:\n\t"
        "s_sub_u32 s22, s65, 28\n\t"
        "s_lshl_b32 s22, s22, 13\n\t"
        "s_add_u32 s21, s21, s22\n\t"
        "v_mov_b32 v18, v17\n\tv_mov_b32 v17, s52\n\tv_mov_b32 v19, s52\n\t"
        "buffer_store_dwordx4 v[16:19], v113, s[8:11], s21 offen sc1\n\t"
        "s_addk_i32 s21, 8192\n\t"
        "v_mov_b32 v60, v24\n\tv_mov_b32 v62, v25\n\t"
        "s_branch L_store\n\t"
        "L_ack:\n\t"
        "s_lshl_b32 s21, %[bid], 3\n\t"
        "s_mov_b64 exec, 1\n\t"
        "buffer_store_dwordx2 v[62:63], off, s[8:11], s21 offset:" STR(ACK_OFF) " sc1\n\t"
        "L_end0:\n\t"
        "s_endpgm\n\t"
        "L_notpub:\n\t"
        "s_cmp_lt_u32 s53, 3\n\t"
        "s_cbranch_scc1 L_end0\n\t"
        "s_cmp_eq_u32 s53, 3\n\t"
        "s_barrier\n\t"
        "s_cbranch_scc1 L_ack\n\t"
        "s_cmp_gt_u32 s53, 7\n\t"
        "s_cbranch_scc1 L_notdown\n\t"
        IFPOST(0) IFPOST(1) IFPOST(2) IFPOST(3) IFPOST(4) IFPOST(5) IFPOST(6) IFPOST(7)
        "v_bfe_u32 v9, v1, 1, 2\n\t"
        "v_cmp_eq_u32 s[22:23], 0, v9\n\tv_cmp_eq_u32 s[56:57], 1, v9\n\tv_cmp_eq_u32 s[58:59], 2, v9\n\tv_cmp_eq_u32 vcc, 3, v9\n\t"
        "v_cndmask_b32 v12, 0, v112, s[22:23]\n\tv_cndmask_b32 v13, 0, v112, s[56:57]\n\t"
        "v_cndmask_b32 v14, 0, v112, s[58:59]\n\tv_cndmask_b32 v15, 0, v112, vcc\n\t"
        "v_and_b32 v5, 56, v1\n\t"
        "v_lshlrev_b32 v5, 2, v5\n\t"
        "s_mul_i32 s21, s19, 4*7168\n\t"
        "s_lshl_b32 s22, s18, 7\n\t"
        "s_add_u32 s21, s21, s22\n\t"
        "s_add_u32 s21, s21, " STR(X2_OFF) "\n\t"
        "v_mul_u32_u24 v4, 7168, v6\n\t"
        "v_and_b32 v7, 7, v1\n\t"
        "v_lshl_add_u32 v4, v7, 4, v4\n\t"
        "s_mov_b32 s22, 0x00ff00ff\n\ts_mov_b32 s23, 0x00ff00ff\n\t"
        "s_barrier\n\t"
        "ds_read_b128 v[16:19], v5 offset:4096\n\t"
        "ds_read_b128 v[20:23], v5 offset:4112\n\t"
        "s_waitcnt lgkmcnt(1)\n\t"
        MKH(0) MKH(1) MM2(0, "0") MKH(2) MM2(1, "v[8:11]") MKH(3) MM2(2, "v[8:11]") "s_waitcnt lgkmcnt(0)\n\t" MKH(4) MM2(3, "v[8:11]")
        MKH(5) MM2(4, "v[8:11]") MKH(6) MM2(5, "v[8:11]") MKH(7) MM2(6, "v[8:11]") "s_nop 0\n\t" MM2(7, "v[8:11]")
        "s_nop 7\n\t"
        "v_permlane32_swap_b32 v8, v9\n\t"
        "v_permlane32_swap_b32 v10, v11\n\t"
        "s_nop 1\n\t"
        "v_add_f32_dpp v60, v9, v8 row_ror:8 row_mask:0xf bank_mask:0xf\n\t"
        "v_add_f32_dpp v62, v11, v10 row_ror:8 row_mask:0xf bank_mask:0xf\n\t"
        "s_mov_b64 exec, s[22:23]\n\t"
        "buffer_store_dwordx4 v[60:63], v4, s[8:11], s21 offen sc1\n\t"
        "s_endpgm\n\t"
        "L_notdown:\n\t"
        "s_mov_b32 s59, 0x10000\n\t"
        "s_cmp_eq_u32 s53, 14\n\t"
        "s_cbranch_scc1 L_poll3\n\t"
        "s_lshl_b32 s17, s17, 1\n\t"
        "s_bfe_u32 s18, s16, 0x10001\n\t"
        "s_add_u32 s17, s17, s18\n\t"
        "s_mul_i32 s18, s17, 7168\n\t"
        "s_add_u32 s18, s18, " STR(X2_OFF) "+3072\n\t"
        "s_add_u32 s4, s12, s18\n\t"
        "s_addc_u32 s5, s13, 0\n\t"
        "s_lshr_b32 s18, s17, 2\n\ts_lshl_b32 s18, s18, 7\n\t"
        "s_and_b32 s19, s17, 1\n\ts_lshl_b32 s19, s19, 6\n\t"
        "s_and_b32 s20, s17, 2\n\ts_lshl_b32 s20, s20, 3\n\t"
        "s_add_u32 s18, s18, s19\n\ts_add_u32 s18, s18, s20\n\t"
        "s_add_u32 s14, s14, s18\n\ts_addc_u32 s15, s15, 0\n\t"
        "v_lshlrev_b32 v3, 1, v1\n\t"
        "L_wait2:\n\t"
        "s_memrealtime s[62:63]\n\t"
        "s_waitcnt lgkmcnt(0)\n\t"
        "s_sub_u32 s62, s62, s33\n\t"
        "s_cmpk_lt_u32 s62, " STR(RDELAY) "\n\t"
        "s_cbranch_scc1 L_wait2\n\t"
        ISS7(16) "s_sleep " STR(SLP2) "\n\t" ISS7(44)
        "L_poll2:\n\t"
        CHK7(16, "L_got2a") ISS7(16) "s_sleep " STR(SLP2) "\n\t"
        CHK7(44, "L_got2b") ISS7(44) "s_sleep " STR(SLP2) "\n\t"
        "s_sub_u32 s59, s59, 1\n\t"
        "s_cbranch_scc0 L_poll2\n\t"
        "s_waitcnt vmcnt(0)\n\t"
        "L_got2a:\n\t" SUM7(16) "s_branch L_fin2\n\t"
        "L_got2b:\n\t" SUM7(44)
        "L_fin2:\n\t"
        "v_add_f32 v8, v8, v9\n\tv_add_f32 v10, v10, v11\n\t"
        "s_nop 1\n\t"
        "v_add_f32_dpp v8, v8, v8 row_ror:8 row_mask:0xf bank_mask:0xf\n\t"
        "v_add_f32_dpp v10, v10, v10 row_ror:8 row_mask:0xf bank_mask:0xf\n\t"
        "s_nop 1\n\t"
        "v_permlane16_swap_b32 v8, v10\n\t"
        "s_nop 0\n\t"
        "v_add_f32 v8, v8, v10\n\t"
        "v_mov_b32 v9, v8\n\t"
        "s_nop 1\n\t"
        "v_permlane32_swap_b32 v8, v9\n\t"
        "s_nop 0\n\t"
        "v_add_f32 v8, v8, v9\n\t"
        "v_cvt_pk_bf16_f32 v8, v8, v8\n\t"
        "s_mov_b64 exec, 0x00ff00ff\n\t"
        "global_store_short v3, v8, s[14:15]\n\t"
        "s_endpgm\n\t"
        "L_poll3:\n\t"
        "s_sleep 16\n\t"
        "buffer_load_dwordx4 v[16:19], v113, s[8:11], 0 offen offset:" STR(ACK_OFF) " sc1\n\t"
        "buffer_load_dwordx4 v[20:23], v113, s[8:11], 0 offen offset:" STR(ACK_OFF) "+1024 sc1\n\t"
        "s_sub_u32 s59, s59, 1\n\t"
        "s_waitcnt vmcnt(0)\n\t"
        X3("v8", 16) X3("v9", 20)
        "v_or_b32 v8, v8, v9\n\t"
        "v_cmp_ne_u32 vcc, 0, v8\n\t"
        "s_cbranch_vccz L_got3\n\t"
        "s_cbranch_scc0 L_poll3\n\t"
        "L_got3:\n\t"
        "s_mov_b64 exec, 1\n\t"
        "buffer_store_dword v63, off, s[8:11], 0 sc1\n\t"
        "s_endpgm\n\t"
        :
        : [ka] "s"(__builtin_amdgcn_kernarg_segment_ptr()), [bid] "s"(blockIdx.x), [tid] "v"(threadIdx.x),
          [lds] "v"((__attribute__((address_space(3))) unsigned*)lds)
        : "memory", "vcc", "scc",
          "v1", "v2", "v3", "v4", "v5", "v6", "v7", "v8", "v9", V10(1), V10(2), V10(3), V10(4), V10(5), V10(6), V10(7), V10(8), V10(9), V10(10),
          "v110", "v111", "v112", "v113", "v114", "v115",
          "s3", "s4", "s5", "s6", "s7", "s8", "s9", S10(1), S10(2), "s30", "s31", "s33", "s34", "s35", "s36", "s37", "s38", "s39",
          S10(4), S10(5), S10(6));
}

extern "C" void kernel_launch(void* const* d_in, const int* in_sizes, int n_in,
                              void* d_out, int out_size, void* d_ws, size_t ws_size,
                              hipStream_t stream) {
    glu_bilinear_decode<<<256, 512, 0, stream>>>(d_in[0], d_in[1], d_in[2], d_in[3], d_ws, d_out);
}
```
